```python
import torch

def _softmax(x, axis=-1):
    return torch.softmax(x, dim=axis)
def _gelu(x):
    return torch.nn.functional.gelu(x, approximate="tanh")
def _silu(x):
    return torch.nn.functional.silu(x)
def _sigmoid(x):
    return torch.sigmoid(x)
def _layernorm(x, axis=-1, eps=1e-5):
    m = x.mean(dim=axis, keepdim=True)
    v = x.var(dim=axis, keepdim=True, unbiased=False)
    return (x - m) / torch.sqrt(v + eps)
def _scatter_add(tgt, idx, src, axis=0):
    return tgt.clone().index_add_(axis, idx, src)
def _segment_sum(src, seg, n):
    out = torch.zeros((n,) + src.shape[1:], dtype=src.dtype)
    return out.index_add_(0, seg, src)
def _pad(x, pw):
    flat = tuple(v for pair in reversed(pw) for v in pair)
    return torch.nn.functional.pad(x, flat)

kernel_name = 'sequential_preLN__ssm_conv_h3__glu_gated_geglu__prompt'

def setup_inputs():

    _w = (lambda a: (lambda t: t.to(torch.bfloat16) if t.is_floating_point() else t)(torch.as_tensor(a)))
    def _A_setup():
        B, S, D = 1, 2048, 640
        ks = 4
        return {
            "q": _w((lambda *s: torch.randn(*s, dtype=torch.bfloat16) * 0.05)(B, S, D)),
            "k": _w((lambda *s: torch.randn(*s, dtype=torch.bfloat16) * 0.2)(B, S, D)),
            "v": _w((lambda *s: torch.randn(*s, dtype=torch.bfloat16) * 0.2)(B, S, D)),
            "shift_k": _w((lambda *s: torch.randn(*s, dtype=torch.bfloat16) * 0.2)(D, ks) * 0.5),
            "ssm_k": _w((lambda *s: torch.randn(*s, dtype=torch.bfloat16) * 0.4)(D, S) * (1.0 / S ** 0.5)),
            "ssm_D": _w((lambda *s: torch.randn(*s, dtype=torch.bfloat16) * 0.2)(D)),
        }
    _A_in = _A_setup()

    def _B_setup():
        B, S, Dm, Df = 1, 2048, 640, 1536
        return {
            "x": _w((lambda *s: torch.randn(*s, dtype=torch.bfloat16) * 0.05)(B, S, Dm)),
            "w_gate": _w((lambda *s: torch.randn(*s, dtype=torch.bfloat16) * 0.4)(Dm, Df) * (Dm ** -0.5)),
            "w_up": _w((lambda *s: torch.randn(*s, dtype=torch.bfloat16) * 0.4)(Dm, Df) * (Dm ** -0.5)),
            "w_down": _w((lambda *s: torch.randn(*s, dtype=torch.bfloat16) * 0.4)(Df, Dm) * (Df ** -0.5)),
        }
    _B_in = _B_setup()

    return {
        "A_q": _A_in["q"],
        "A_k": _A_in["k"],
        "A_v": _A_in["v"],
        "A_shift_k": _A_in["shift_k"],
        "A_ssm_k": _A_in["ssm_k"],
        "A_ssm_D": _A_in["ssm_D"],
        "B_x": _B_in["x"],
        "B_w_gate": _B_in["w_gate"],
        "B_w_up": _B_in["w_up"],
        "B_w_down": _B_in["w_down"],
    }

def reference(A_q, A_k, A_v, A_shift_k, A_ssm_k, A_ssm_D, B_x, B_w_gate, B_w_up, B_w_down):
    def _A_ref(q, k, v, shift_k, ssm_k, ssm_D):
        B, S, D = q.shape
        ks = shift_k.shape[-1]
        k_pad = _pad(k, ((0, 0), (ks - 1, 0), (0, 0)))
        k_shift = torch.zeros_like(k)
        for t in range(ks):
            k_shift = k_shift + k_pad[:, t:t + S, :] * shift_k[:, ks - 1 - t][None, None, :]
        kv = k_shift * v
        L = 2 * S
        kv_pad = _pad(kv, ((0, 0), (0, S), (0, 0)))
        kf_pad = _pad(ssm_k, ((0, 0), (0, S)))
        kv_f = (lambda x, axes=(-2,-1): torch.fft.rfft2(x.float(), dim=axes))(kv_pad[:, None, :, :], axes=(1, 2))
        kf_f = (lambda x, axes=(-2,-1): torch.fft.rfft2(x.float(), dim=axes))(kf_pad[:, None, :], axes=(1, 2))
        kf_f = (lambda x, *d: x.permute(*d))(kf_f, 1, 2, 0)
        y_f = kv_f * kf_f[None, :, :, :]
        y = (lambda x, s, axes=(-2,-1): torch.fft.irfft2(x, s=s, dim=axes).to(torch.bfloat16))(y_f, (1, L), axes=(1, 2))[:, 0, :S, :]
        y = y + kv * ssm_D[None, None, :]
        return q * y

    def _B_ref(x, w_gate, w_up, w_down):
        g = torch.einsum("bsd,df->bsf", x, w_gate)
        u = torch.einsum("bsd,df->bsf", x, w_up)
        h = _gelu(g) * u
        return torch.einsum("bsf,fd->bsd", h, w_down)

    x = A_q
    h = x + _A_ref(_layernorm(x, axis=-1), A_k, A_v, A_shift_k, A_ssm_k, A_ssm_D)
    h2 = h + _B_ref(_layernorm(h, axis=-1), B_w_gate, B_w_up, B_w_down)
    return h2
```

```cpp
#include <hip/hip_runtime.h>
#include <hip/hip_bf16.h>
typedef unsigned short u16;
__device__ __forceinline__ float bf2f(u16 x){ return __uint_as_float(((unsigned)x)<<16); }
__device__ __forceinline__ u16 f2bf(float f){ __bf16 b=(__bf16)f; return __builtin_bit_cast(u16,b); }
__device__ __forceinline__ float rbf(float f){ return bf2f(f2bf(f)); }
constexpr int S=2048, D=640, F=1536;

__global__ void k_kv(const u16* k,const u16* v,const u16* sh,u16* kv){
  int i=blockIdx.x*256+threadIdx.x; if(i>=S*D) return; int s=i/D,d=i%D;
  float acc=0.f;
  for(int t=0;t<4;++t){ int ss=s+t-3; float kk= ss>=0? bf2f(k[ss*D+d]):0.f; acc=rbf(acc+rbf(kk*bf2f(sh[d*4+3-t]))); }
  kv[i]=f2bf(acc*bf2f(v[i]));
}
__global__ void k_conv(const u16* kv,const u16* sk,const u16* sD,u16* y){
  int i=blockIdx.x*256+threadIdx.x; if(i>=S*D) return; int s=i/D,d=i%D;
  float acc=0.f;
  for(int t=0;t<=s;++t) acc+=bf2f(kv[t*D+d])*bf2f(sk[d*S+s-t]);
  float yy=rbf(acc); yy=rbf(yy+rbf(bf2f(kv[i])*bf2f(sD[d])));
  y[i]=f2bf(yy);
}
__device__ float blk_sum(float x,float* sm){
  for(int o=32;o>0;o>>=1) x+=__shfl_xor(x,o);
  __syncthreads();
  if((threadIdx.x&63)==0) sm[threadIdx.x>>6]=x;
  __syncthreads();
  float r=0; for(int i=0;i<(int)blockDim.x/64;++i) r+=sm[i];
  return r;
}
__global__ void k_ln(const u16* q,const u16* y,u16* h,u16* hln){
  __shared__ float sm[16];
  int s=blockIdx.x,d=threadIdx.x;
  float x=bf2f(q[s*D+d]);
  float m=blk_sum(x,sm)/D; float v=blk_sum((x-m)*(x-m),sm)/D;
  float mb=rbf(m), den=rbf(sqrtf(rbf(rbf(v)+1e-5f)));
  float ql=rbf(rbf(x-mb)/den);
  float hh=rbf(x+rbf(ql*bf2f(y[s*D+d])));
  h[s*D+d]=f2bf(hh);
  m=blk_sum(hh,sm)/D; v=blk_sum((hh-m)*(hh-m),sm)/D;
  mb=rbf(m); den=rbf(sqrtf(rbf(rbf(v)+1e-5f)));
  hln[s*D+d]=f2bf(rbf(hh-mb)/den);
}
__global__ void k_g1(const u16* hln,const u16* wg,const u16* wu,u16* hh){
  int i=blockIdx.x*256+threadIdx.x; int s=i/F,f=i%F;
  float g=0,u=0;
  for(int d=0;d<D;++d){ float a=bf2f(hln[s*D+d]); g+=a*bf2f(wg[d*F+f]); u+=a*bf2f(wu[d*F+f]); }
  g=rbf(g);u=rbf(u);
  float ge=0.5f*g*(1.f+tanhf(0.7978845608028654f*(g+0.044715f*g*g*g)));
  hh[i]=f2bf(rbf(ge)*u);
}
__global__ void k_g2(const u16* hh,const u16* wd,const u16* h,u16* out){
  int i=blockIdx.x*256+threadIdx.x; int s=i/D,d=i%D;
  float a=0; for(int f=0;f<F;++f) a+=bf2f(hh[s*F+f])*bf2f(wd[f*D+d]);
  out[i]=f2bf(bf2f(h[i])+rbf(a));
}
extern "C" void kernel_launch(void* const* d_in, const int* in_sizes, int n_in, void* d_out, int out_size, void* d_ws, size_t ws_size, hipStream_t stream) {
  const u16* q=(const u16*)d_in[0],*k=(const u16*)d_in[1],*v=(const u16*)d_in[2],*sh=(const u16*)d_in[3],*sk=(const u16*)d_in[4],*sD=(const u16*)d_in[5];
  const u16* wg=(const u16*)d_in[7],*wu=(const u16*)d_in[8],*wd=(const u16*)d_in[9];
  u16* ws=(u16*)d_ws; u16* kv=ws; u16* y=ws+S*D; u16* h=y+S*D; u16* hln=h+S*D; u16* hh=hln+S*D;
  k_kv<<<S*D/256,256,0,stream>>>(k,v,sh,kv);
  k_conv<<<S*D/256,256,0,stream>>>(kv,sk,sD,y);
  k_ln<<<S,640,0,stream>>>(q,y,h,hln);
  k_g1<<<S*F/256,256,0,stream>>>(hln,wg,wu,hh);
  k_g2<<<S*D/256,256,0,stream>>>(hh,wd,h,(u16*)d_out);
}
```

```cpp
#include <hip/hip_runtime.h>

typedef unsigned short u16;
typedef unsigned int u32;
typedef short s16x4 __attribute__((ext_vector_type(4)));
typedef __bf16 bf16x8 __attribute__((ext_vector_type(8)));
typedef __bf16 bf16x2 __attribute__((ext_vector_type(2)));
typedef float f32x2 __attribute__((ext_vector_type(2)));
typedef float f32x4 __attribute__((ext_vector_type(4)));
typedef float f32x16 __attribute__((ext_vector_type(16)));
typedef u32 u32x2 __attribute__((ext_vector_type(2)));
typedef u32 u32x4 __attribute__((ext_vector_type(4)));
#define LDS_AS __attribute__((address_space(3)))
#define GLB_AS __attribute__((address_space(1)))

constexpr int S_ = 2048, D_ = 640, F_ = 1536;
constexpr int KVP = 2336;

constexpr size_t WS_XP = 0;
constexpr size_t WS_KVR = WS_XP + (size_t)D_ * S_ * 2;
constexpr size_t WS_ST1 = WS_KVR + (size_t)D_ * KVP * 2 + 4096;
constexpr size_t WS_HT = WS_ST1 + 16384;
constexpr size_t WS_CS = WS_HT + (size_t)D_ * S_ * 2;
constexpr size_t WS_WDP = WS_CS + 16384;
constexpr size_t WS_HHF = WS_WDP + (size_t)F_ * D_ * 2;

__device__ __forceinline__ float bflo(u32 x) { return __uint_as_float(x << 16); }
__device__ __forceinline__ float bfhi(u32 x) { return __uint_as_float(x & 0xffff0000u); }
__device__ __forceinline__ u32 pack2(float lo, float hi) {
  bf16x2 v; v.x = (__bf16)lo; v.y = (__bf16)hi;
  return __builtin_bit_cast(u32, v);
}
__device__ __forceinline__ float rbf(float f) { __bf16 b = (__bf16)f; return (float)b; }

__device__ __forceinline__ void glds16(const void* g, LDS_AS char* l) {
  __builtin_amdgcn_global_load_lds((const GLB_AS void*)g, (LDS_AS void*)l, 16, 0, 0);
}
__device__ __forceinline__ u32x2 tr_read(LDS_AS char* p) {
  s16x4 v = __builtin_amdgcn_ds_read_tr16_b64_v4i16((LDS_AS s16x4*)p);
  return __builtin_bit_cast(u32x2, v);
}
__device__ __forceinline__ bf16x8 mk8(u32x2 a, u32x2 b) {
  u32x4 v = {a.x, a.y, b.x, b.y};
  return __builtin_bit_cast(bf16x8, v);
}
#define WAIT_VM(n) asm volatile("s_waitcnt vmcnt(%0)" ::"n"(n) : "memory")
#define WAIT_LGKM0() asm volatile("s_waitcnt lgkmcnt(0)" ::: "memory")
#define SBAR() __builtin_amdgcn_s_barrier()
#define SCHED0() __builtin_amdgcn_sched_barrier(0)

__global__ __launch_bounds__(256) void k0_prep(const u16* __restrict__ q, const u16* __restrict__ k,
                                               const u16* __restrict__ v, const u16* __restrict__ shk,
                                               char* __restrict__ ws) {
  __shared__ __attribute__((aligned(16))) char smraw[8192 + 8192 + 9216];
  LDS_AS char* sm = (LDS_AS char*)smraw;
  const int bid = blockIdx.x, tid = threadIdx.x, lane = tid & 63;
  const int w = __builtin_amdgcn_readfirstlane(tid >> 6);
  if (bid < 320) {
    const int rb = bid / 10, cb = bid % 10, s0 = rb * 64, c0 = cb * 64;
    {
      const int r8 = lane >> 3, unit = (lane >> 1) & 3, half = lane & 1;
#pragma unroll
      for (int j = 0; j < 2; ++j) {
        const int it = w + 4 * j, row = it * 8 + r8;
        const int f = (row >> 2) & 3;
        glds16(q + (size_t)(s0 + row) * D_ + c0 + 16 * (unit ^ f) + 8 * half, sm + it * 1024);
      }
#pragma unroll
      for (int j = 0; j < 2; ++j) {
        const int it = w + 4 * j, row = it * 8 + r8;
        const int f = ((row >> 1) & 1) | (((row >> 3) & 1) << 1);
        glds16(v + (size_t)(s0 + row) * D_ + c0 + 16 * (unit ^ f) + 8 * half, sm + 8192 + it * 1024);
      }
#pragma unroll
      for (int j = 0; j < 3; ++j) {
        const int it = w + 4 * j;
        if (it < 9) {
          const int row = it * 8 + r8;
          const int f = ((row >> 1) & 1) | (((row >> 3) & 1) << 1);
          int sr = s0 - 8 + row; sr = sr < 0 ? 0 : sr;
          glds16(k + (size_t)sr * D_ + c0 + 16 * (unit ^ f) + 8 * half, sm + 16384 + it * 1024);
        }
      }
    }
    const int i = lane & 15, g = lane >> 4, qq = (lane & 15) >> 2, pp = lane & 3;
    const int c = c0 + 16 * w + i;
    const u32x2 shv = *(const u32x2*)(shk + c * 4);
    const float w0 = bflo(shv.x), w1 = bfhi(shv.x), w2 = bflo(shv.y), w3 = bfhi(shv.y);
    if (rb == 0) {
      u16* kvR = (u16*)(ws + WS_KVR);
#pragma unroll
      for (int j = 0; j < 8; ++j) {
        const int id = tid + 256 * j, ch = id >> 5, cc = id & 31;
        *(u32x4*)(kvR + (size_t)(c0 + ch) * KVP + S_ + cc * 8) = (u32x4){0u, 0u, 0u, 0u};
      }
    }
    WAIT_VM(0);
    __syncthreads();
    u16* xP = (u16*)(ws + WS_XP);
    u16* kvR = (u16*)(ws + WS_KVR);
#pragma unroll
    for (int sbi = 0; sbi < 2; ++sbi) {
      const int sb = sbi * 32;
      u32x2 xa[2];
#pragma unroll
      for (int t = 0; t < 2; ++t) {
        const int row = sb + 16 * (g >> 1) + 4 * (g & 1) + 2 * t + (qq & 1) + 8 * (qq >> 1);
        const int f = (row >> 2) & 3;
        xa[t] = tr_read(sm + row * 128 + 32 * (w ^ f) + 8 * pp);
      }
      *(u32x4*)(xP + (size_t)c * S_ + s0 + sb + 8 * g) = (u32x4){xa[0].x, xa[0].y, xa[1].x, xa[1].y};
      u32x2 va[2], ka[3];
#pragma unroll
      for (int t = 0; t < 2; ++t) {
        const int row = sb + 8 * g + 4 * t + qq;
        const int f = ((row >> 1) & 1) | (((row >> 3) & 1) << 1);
        va[t] = tr_read(sm + 8192 + row * 128 + 32 * (w ^ f) + 8 * pp);
      }
#pragma unroll
      for (int t = 0; t < 3; ++t) {
        const int row = sb + 8 * g + 4 + 4 * t + qq;
        const int f = ((row >> 1) & 1) | (((row >> 3) & 1) << 1);
        ka[t] = tr_read(sm + 16384 + row * 128 + 32 * (w ^ f) + 8 * pp);
      }
      if (s0 + sb + 8 * g == 0) { ka[0].x = 0u; ka[0].y = 0u; }
      float kf[12], vf[8], o[8];
#pragma unroll
      for (int t = 0; t < 3; ++t) {
        kf[4 * t] = bflo(ka[t].x); kf[4 * t + 1] = bfhi(ka[t].x); kf[4 * t + 2] = bflo(ka[t].y); kf[4 * t + 3] = bfhi(ka[t].y);
      }
#pragma unroll
      for (int t = 0; t < 2; ++t) {
        vf[4 * t] = bflo(va[t].x); vf[4 * t + 1] = bfhi(va[t].x); vf[4 * t + 2] = bflo(va[t].y); vf[4 * t + 3] = bfhi(va[t].y);
      }
#pragma unroll
      for (int e = 0; e < 8; ++e) {
        float a = kf[e + 1] * w3;
        a = fmaf(kf[e + 2], w2, a);
        a = fmaf(kf[e + 3], w1, a);
        a = fmaf(kf[e + 4], w0, a);
        o[e] = a * vf[e];
      }
      u32x4 ov = {pack2(o[7], o[6]), pack2(o[5], o[4]), pack2(o[3], o[2]), pack2(o[1], o[0])};
      *(u32x4*)(kvR + (size_t)c * KVP + 2040 - (s0 + sb + 8 * g)) = ov;
    }
  } else {
    const int s = (bid - 320) * 16 + (tid >> 4), l16 = tid & 15;
    u32x4 d[5];
#pragma unroll
    for (int j = 0; j < 5; ++j) d[j] = *(const u32x4*)(q + (size_t)s * D_ + (j * 16 + l16) * 8);
    float sum = 0.f;
#pragma unroll
    for (int j = 0; j < 5; ++j)
#pragma unroll
      for (int e = 0; e < 4; ++e) sum += bflo(d[j][e]) + bfhi(d[j][e]);
#pragma unroll
    for (int o = 1; o < 16; o <<= 1) sum += __shfl_xor(sum, o);
    const float mean = sum * (1.f / D_);
    float sq = 0.f;
#pragma unroll
    for (int j = 0; j < 5; ++j)
#pragma unroll
      for (int e = 0; e < 4; ++e) {
        float a = bflo(d[j][e]) - mean, b = bfhi(d[j][e]) - mean;
        sq = fmaf(a, a, sq); sq = fmaf(b, b, sq);
      }
#pragma unroll
    for (int o = 1; o < 16; o <<= 1) sq += __shfl_xor(sq, o);
    if (l16 == 0) {
      const float mb = rbf(mean);
      const float den = rbf(sqrtf(rbf(rbf(sq * (1.f / D_)) + 1e-5f)));
      const float r = 1.f / den;
      const int n = s >> 8, A = (s >> 6) & 3, qd = (s >> 4) & 3, s16 = s & 15;
      const int reg = (s16 & 1) | ((s16 >> 3) << 1), ap = (s16 >> 1) & 3;
      const int ln = 4 * A + ap + 16 * qd;
      float* st = (float*)(ws + WS_ST1);
      st[((n * 2 + 0) * 64 + ln) * 4 + reg] = r;
      st[((n * 2 + 1) * 64 + ln) * 4 + reg] = -mb * r;
    }
  }
}

constexpr int K1_FA0 = 4224, K1_GB = 8448, K1_GP = 4640, K1_WAVE = 8448 + 19 * 1024;
constexpr int K1_LDS = 4 * K1_WAVE;

__global__ __launch_bounds__(256, 1) void k1_conv(const u16* __restrict__ ssk, const u16* __restrict__ ssD,
                                                  const u16* __restrict__ wg, const u16* __restrict__ wu,
                                                  const u16* __restrict__ wd, char* __restrict__ ws) {
  extern __shared__ __attribute__((aligned(16))) char smraw[];
  LDS_AS char* sm = (LDS_AS char*)smraw;
  const int bid = blockIdx.x, tid = threadIdx.x, lane = tid & 63;
  const int w = __builtin_amdgcn_readfirstlane(tid >> 6);
  if (bid < 160) {
    const int c = bid * 4 + w;
    LDS_AS char* wb = sm + w * K1_WAVE;
    const u16* krow = ssk + (size_t)c * S_;
    u32x4 kd[4]; u32 kp[4];
#pragma unroll
    for (int j = 0; j < 4; ++j) {
      const int m = 64 * j + lane;
      kd[j] = *(const u32x4*)(krow + 8 * m);
      kp[j] = *(const u32*)(krow + (m > 0 ? 8 * m - 2 : 0));
    }
    {
      const u16* kvrow = (const u16*)(ws + WS_KVR) + (size_t)c * KVP;
#pragma unroll
      for (int it = 0; it < 19; ++it) {
        const int id = it * 64 + lane;
        int cp = id / 290; const int ch = id - cp * 290; cp = cp > 3 ? 3 : cp;
        glds16(kvrow + 8 * ch + 6 - 2 * cp, wb + K1_GB + it * 1024);
      }
    }
    const int a = lane & 15, qd = lane >> 4;
    const u16* xrow = (const u16*)(ws + WS_XP) + (size_t)c * S_;
    u32x2 xv[8];
#pragma unroll
    for (int n = 0; n < 8; ++n) xv[n] = *(const u32x2*)(xrow + 256 * n + 64 * (a >> 2) + 16 * qd + 4 * (a & 3));
    const u32 Dv = ssD[c];
    if (lane < 14) {
      const int o = lane < 7 ? lane * 16 : K1_FA0 + (lane - 7) * 16;
      *(LDS_AS u32x4*)(wb + o) = (u32x4){0u, 0u, 0u, 0u};
    }
    if (lane == 0) kp[0] = 0u;
#pragma unroll
    for (int j = 0; j < 4; ++j) {
      const int m = 64 * j + lane;
      *(LDS_AS u32x4*)(wb + 16 * (m + 7)) = kd[j];
      u32x4 sh = {__builtin_amdgcn_alignbit(kd[j].x, kp[j], 16), __builtin_amdgcn_alignbit(kd[j].y, kd[j].x, 16),
                  __builtin_amdgcn_alignbit(kd[j].z, kd[j].y, 16), __builtin_amdgcn_alignbit(kd[j].w, kd[j].z, 16)};
      *(LDS_AS u32x4*)(wb + K1_FA0 + 16 * (m + 7)) = sh;
    }
    WAIT_VM(0);
    WAIT_LGKM0();
    LDS_AS char* fb = wb + ((a & 1) ? 0 : K1_FA0) + 16 * ((a >> 1) + qd);
    LDS_AS char* gb = wb + K1_GB + K1_GP * (a & 3) + 16 * (qd - 8 * (a >> 2) + 24);
    f32x4 acc[8];
#pragma unroll
    for (int n = 0; n < 8; ++n) acc[n] = (f32x4){0.f, 0.f, 0.f, 0.f};
    const u32 dsel = (a & 1) ? Dv : (Dv << 16);
#pragma unroll
    for (int r = 0; r < 8; ++r) {
      bf16x8 Ff[8], Gf[8];
#pragma unroll
      for (int kk = 0; kk < 8; ++kk) {
        Gf[kk] = *(LDS_AS bf16x8*)(gb + 3584 - 512 * kk + 64 * r);
        Ff[kk] = *(LDS_AS bf16x8*)(fb + 64 * (8 * kk + r));
      }
#pragma unroll
      for (int kk = 0; kk < 8; ++kk)
#pragma unroll
        for (int m = 0; m < 8 - kk; ++m)
          acc[kk + m] = __builtin_amdgcn_mfma_f32_16x16x32_bf16(Ff[kk], Gf[m], acc[kk + m], 0, 0, 0);
      if (r < 2) {
        u32x4 fd = {((a >> 1) + qd == 7 - 4 * r) ? dsel : 0u, 0u, 0u, 0u};
        const bf16x8 FD = __builtin_bit_cast(bf16x8, fd);
#pragma unroll
        for (int m = 0; m < 8; ++m) acc[m] = __builtin_amdgcn_mfma_f32_16x16x32_bf16(FD, Gf[m], acc[m], 0, 0, 0);
      }
    }
    const float* st = (const float*)(ws + WS_ST1);
    u16* hrow = (u16*)(ws + WS_HT) + (size_t)c * S_;
#pragma unroll
    for (int n = 0; n < 8; ++n) {
      const f32x4 rr = *(const f32x4*)(st + ((n * 2 + 0) * 64 + lane) * 4);
      const f32x4 mm = *(const f32x4*)(st + ((n * 2 + 1) * 64 + lane) * 4);
      const f32x4 xf = {bflo(xv[n].x), bfhi(xv[n].x), bflo(xv[n].y), bfhi(xv[n].y)};
      const f32x4 ql = xf * rr + mm;
      const f32x4 h = ql * acc[n] + xf;
      u32x2 o = {pack2(h.x, h.y), pack2(h.z, h.w)};
      *(u32x2*)(hrow + 256 * n + 64 * (a >> 2) + 16 * qd + 4 * (a & 3)) = o;
    }
  } else if (bid < 208) {
    const int j = bid - 160;
    const u16* W = (j < 24 ? wg : wu) + (j % 24) * 64;
    const int cc = tid & 7, rg = tid >> 3;
    float s[8];
#pragma unroll
    for (int e = 0; e < 8; ++e) s[e] = 0.f;
    u32x4 d[20];
#pragma unroll
    for (int it = 0; it < 20; ++it) d[it] = *(const u32x4*)(W + (size_t)(it * 32 + rg) * F_ + cc * 8);
#pragma unroll
    for (int it = 0; it < 20; ++it)
#pragma unroll
      for (int e = 0; e < 4; ++e) { s[2 * e] += bflo(d[it][e]); s[2 * e + 1] += bfhi(d[it][e]); }
    LDS_AS float* red = (LDS_AS float*)sm;
#pragma unroll
    for (int e = 0; e < 8; ++e) red[rg * 65 + cc * 8 + e] = s[e];
    __syncthreads();
    if (tid < 64) {
      float t = 0.f;
#pragma unroll
      for (int r = 0; r < 32; ++r) t += red[r * 65 + tid];
      ((float*)(ws + WS_CS))[j * 64 + tid] = t;
    }
  } else {
    const int ks = bid - 208;
    const u16* src = wd + (size_t)ks * 32 * D_;
#pragma unroll
    for (int it = 0; it < 10; ++it) {
      const int I = w * 10 + it, id = I * 64 + lane, row = id / 80, ch = id - row * 80;
      const int unit = (ch >> 1) ^ (row & 7);
      glds16(src + row * D_ + unit * 16 + 8 * (ch & 1), sm + I * 1024);
    }
    WAIT_VM(0);
    __syncthreads();
    const int g = lane >> 4, qq = (lane & 15) >> 2, pp = lane & 3;
    u16* dst = (u16*)(ws + WS_WDP);
#pragma unroll
    for (int it = 0; it < 10; ++it) {
      const int dt = w * 10 + it;
      u32x2 a[2];
#pragma unroll
      for (int t = 0; t < 2; ++t) {
        const int row = 16 * (g >> 1) + 4 * (g & 1) + 8 * t + qq;
        a[t] = tr_read(sm + row * 1280 + 32 * (dt ^ (row & 7)) + 8 * pp);
      }
      *(u32x4*)(dst + ((size_t)(dt * 48 + ks) * 64 + lane) * 8) = (u32x4){a[0].x, a[0].y, a[1].x, a[1].y};
    }
  }
}

constexpr int K2_STAGE = 40960, K2_NS = 4, K2_LDS = K2_STAGE * K2_NS, K2_NT = 10;

__global__ __launch_bounds__(512, 2) void k2_gemm1(const u16* __restrict__ wg, const u16* __restrict__ wu,
                                                   char* __restrict__ ws) {
  extern __shared__ __attribute__((aligned(16))) char smraw[];
  LDS_AS char* sm = (LDS_AS char*)smraw;
  const int bid = blockIdx.x, tid = threadIdx.x, lane = tid & 63;
  const int w = __builtin_amdgcn_readfirstlane(tid >> 6);
  const int xcd = bid & 7, jj = bid >> 3;
  const int pm = 8 * (xcd & 1) + (jj & 7), fn = 4 * (xcd >> 1) + (jj >> 3);
  if (w >= 4) {
    const int hw = w - 4;
    const char* hsrc = ws + WS_HT + (size_t)(16 * hw) * (S_ * 2) + pm * 256;
    const char* gsrc = (const char*)wg + (size_t)(16 * hw) * (F_ * 2) + fn * 192;
    const char* usrc = (const char*)wu + (size_t)(16 * hw) * (F_ * 2) + fn * 192;
    const u32 hoff = (lane >> 4) * (S_ * 2) + 16 * ((lane & 15) ^ ((lane >> 4) << 2));
    u32 goff[3], uoff[3];
#pragma unroll
    for (int i = 0; i < 3; ++i) {
      const int id = 64 * i + lane, row = id / 12, ch = id - row * 12;
      goff[i] = row * (F_ * 2) + 16 * ch;
      uoff[i] = row * (F_ * 2) + 16 * (ch ^ 2);
    }
    auto stage = [&](int kt, int buf) {
      LDS_AS char* b = sm + buf * K2_STAGE;
      const char* hs = hsrc + (size_t)kt * (64 * S_ * 2);
      const char* gs = gsrc + (size_t)kt * (64 * F_ * 2);
      const char* us = usrc + (size_t)kt * (64 * F_ * 2);
#pragma unroll
      for (int i = 0; i < 4; ++i) glds16(hs + (size_t)i * (4 * S_ * 2) + hoff, b + (4 * hw + i) * 1024);
#pragma unroll
      for (int i = 0; i < 3; ++i) glds16(gs + goff[i], b + 16384 + (3 * hw + i) * 1024);
#pragma unroll
      for (int i = 0; i < 3; ++i) glds16(us + uoff[i], b + 28672 + (3 * hw + i) * 1024);
    };
    stage(0, 0); stage(1, 1); stage(2, 2);
    f32x2 sum[4], sq[4];
#pragma unroll
    for (int e = 0; e < 4; ++e) { sum[e] = (f32x2){0.f, 0.f}; sq[e] = (f32x2){0.f, 0.f}; }
    const u32 soff = (4 * hw + (lane >> 4)) * 256 + 16 * ((lane & 15) ^ ((lane >> 4) << 2));
#pragma unroll
    for (int t = 0; t < K2_NT; ++t) {
      if (t < K2_NT - 2) WAIT_VM(20); else if (t == K2_NT - 2) WAIT_VM(10); else WAIT_VM(0);
      SBAR();
      if (t + 3 < K2_NT) stage(t + 3, (t + 3) & 3);
      u32x4 d[4];
      const u32 base = (u32)(size_t)(sm + (t & 3) * K2_STAGE) + soff;
      asm volatile("ds_read_b128 %0, %4\n\tds_read_b128 %1, %4 offset:4096\n\tds_read_b128 %2, %4 offset:8192\n\t"
                   "ds_read_b128 %3, %4 offset:12288\n\ts_waitcnt lgkmcnt(0)"
                   : "=&v"(d[0]), "=&v"(d[1]), "=&v"(d[2]), "=&v"(d[3]) : "v"(base) : "memory");
#pragma unroll
      for (int j = 0; j < 4; ++j)
#pragma unroll
        for (int e = 0; e < 4; ++e) {
          const f32x2 x = {bflo(d[j][e]), bfhi(d[j][e])};
          sum[e] += x; sq[e] = x * x + sq[e];
        }
    }
    float vals[16];
#pragma unroll
    for (int e = 0; e < 4; ++e) { vals[4 * e] = sum[e].x; vals[4 * e + 1] = sq[e].x; vals[4 * e + 2] = sum[e].y; vals[4 * e + 3] = sq[e].y; }
#pragma unroll
    for (int e = 0; e < 16; ++e) { vals[e] += __shfl_xor(vals[e], 16); vals[e] += __shfl_xor(vals[e], 32); }
    {
      const int e = lane >> 4;
      f32x4 o;
      o.x = e == 0 ? vals[0] : e == 1 ? vals[4] : e == 2 ? vals[8] : vals[12];
      o.y = e == 0 ? vals[1] : e == 1 ? vals[5] : e == 2 ? vals[9] : vals[13];
      o.z = e == 0 ? vals[2] : e == 1 ? vals[6] : e == 2 ? vals[10] : vals[14];
      o.w = e == 0 ? vals[3] : e == 1 ? vals[7] : e == 2 ? vals[11] : vals[15];
      *(LDS_AS f32x4*)(sm + hw * 1024 + (8 * (lane & 15) + 2 * e) * 8) = o;
    }
    WAIT_LGKM0();
    SBAR();
  } else {
    const int wp = w & 1, wf = w >> 1;
    const int g = lane >> 4, blk = g & 1, hs = g >> 1, qq = (lane & 15) >> 2, pp = lane & 3, hi = lane >> 5;
    const float* cs = (const float*)(ws + WS_CS);
    f32x4 csg[3][2], csu[3][2];
#pragma unroll
    for (int tt = 0; tt < 3; ++tt)
#pragma unroll
      for (int h2 = 0; h2 < 2; ++h2) {
        const int f = fn * 96 + 16 * (3 * wf + tt) + 4 * hi + 8 * h2;
        csg[tt][h2] = *(const f32x4*)(cs + f);
        csu[tt][h2] = *(const f32x4*)(cs + F_ + f);
      }
    u32 aoff[3], boff[2];
#pragma unroll
    for (int tt = 0; tt < 3; ++tt) {
      const int t = 3 * wf + tt;
      aoff[tt] = (blk ? 28672 : 16384) + 192 * (8 * hs + qq) + 32 * (blk ? (t ^ 1) : t) + 8 * pp;
    }
#pragma unroll
    for (int j = 0; j < 2; ++j) {
      const int ch = 4 * (2 * wp + j) + 2 * blk + (pp >> 1);
      boff[j] = 256 * (8 * hs + qq) + 16 * (ch ^ (qq << 2)) + 8 * (pp & 1);
    }
    f32x16 acc[3][2];
#pragma unroll
    for (int tt = 0; tt < 3; ++tt)
#pragma unroll
      for (int j = 0; j < 2; ++j)
#pragma unroll
        for (int e = 0; e < 16; ++e) acc[tt][j][e] = 0.f;
    bf16x8 A[2][3], B[2][2];
    auto ldfrag = [&](int set, int buf, int ks) {
      LDS_AS char* b = sm + buf * K2_STAGE;
#pragma unroll
      for (int j = 0; j < 2; ++j)
        B[set][j] = mk8(tr_read(b + boff[j] + 256 * (16 * ks)), tr_read(b + boff[j] + 256 * (16 * ks + 4)));
#pragma unroll
      for (int tt = 0; tt < 3; ++tt)
        A[set][tt] = mk8(tr_read(b + aoff[tt] + 192 * (16 * ks)), tr_read(b + aoff[tt] + 192 * (16 * ks + 4)));
    };
    SBAR();
    ldfrag(0, 0, 0);
#pragma unroll
    for (int t = 0; t < K2_NT; ++t) {
#pragma unroll
      for (int ks = 0; ks < 4; ++ks) {
        const int cur = ks & 1;
        if (ks < 3) {
          ldfrag(cur ^ 1, t & 3, ks + 1);
        } else if (t + 1 < K2_NT) {
          WAIT_LGKM0();
          SCHED0();
          SBAR();
          SCHED0();
          ldfrag(cur ^ 1, (t + 1) & 3, 0);
        }
#pragma unroll
        for (int tt = 0; tt < 3; ++tt)
#pragma unroll
          for (int j = 0; j < 2; ++j)
            acc[tt][j] = __builtin_amdgcn_mfma_f32_32x32x16_bf16(A[cur][tt], B[cur][j], acc[tt][j], 0, 0, 0);
        SCHED0();
      }
    }
    SBAR();
    asm volatile("" ::: "memory");
    u16* hh = (u16*)(ws + WS_HHF);
#pragma unroll
    for (int j = 0; j < 2; ++j) {
      const int pl = 64 * wp + 32 * j + (lane & 31);
      float s1 = 0.f, s2 = 0.f;
#pragma unroll
      for (int h4 = 0; h4 < 4; ++h4) {
        const f32x2 pv = *(LDS_AS f32x2*)(sm + h4 * 1024 + pl * 8);
        s1 += pv.x; s2 += pv.y;
      }
      const float mean = s1 * (1.f / D_);
      const float var = fmaxf(s2 * (1.f / D_) - mean * mean, 0.f);
      const float nmu = -rbf(mean);
      const float den = rbf(sqrtf(rbf(rbf(var) + 1e-5f)));
      const float r = 1.f / den;
      const float k0 = -2.f * 0.7978845608028654f * 1.4426950408889634f;
      const float K0 = k0 * r, K1 = k0 * 0.044715f * r * r * r, ir2 = den * den;
      const int P = pm * 128 + pl;
#pragma unroll
      for (int tt = 0; tt < 3; ++tt) {
        u32 o[4];
#pragma unroll
        for (int i2 = 0; i2 < 4; ++i2) {
          const int i = 2 * i2;
          const f32x2 cg = {csg[tt][i >> 2][i & 3], csg[tt][i >> 2][(i & 3) + 1]};
          const f32x2 cu = {csu[tt][i >> 2][i & 3], csu[tt][i >> 2][(i & 3) + 1]};
          const f32x2 G = cg * nmu + (f32x2){acc[tt][j][i], acc[tt][j][i + 1]};
          const f32x2 U = cu * nmu + (f32x2){acc[tt][j][8 + i], acc[tt][j][8 + i + 1]};
          const f32x2 wv = ((G * G) * K1 + K0) * G;
          f32x2 e; e.x = __builtin_amdgcn_exp2f(wv.x); e.y = __builtin_amdgcn_exp2f(wv.y);
          const f32x2 dd = e * ir2 + ir2;
          f32x2 rc; rc.x = __builtin_amdgcn_rcpf(dd.x); rc.y = __builtin_amdgcn_rcpf(dd.y);
          const f32x2 hv = (G * U) * rc;
          o[i2] = pack2(hv.x, hv.y);
        }
        const int T = fn * 6 + 3 * wf + tt;
        *(u32x4*)(hh + ((size_t)(((P >> 4) * 48 + (T >> 1)) * 4 + 2 * (T & 1) + hi) * 16 + (P & 15)) * 8) =
            (u32x4){o[0], o[1], o[2], o[3]};
      }
    }
  }
}

constexpr int K3_LDS = 4 * 15 * 1024 + 10240;

__global__ __launch_bounds__(256, 1) void k3_gemm2(char* __restrict__ ws, u16* __restrict__ out) {
  extern __shared__ __attribute__((aligned(16))) char smraw[];
  LDS_AS char* sm = (LDS_AS char*)smraw;
  const int bid = blockIdx.x, tid = threadIdx.x, lane = tid & 63;
  const int w = __builtin_amdgcn_readfirstlane(tid >> 6);
  const int xcd = bid & 7, jj = bid >> 3;
  const int pt = 8 * (xcd >> 1) + (jj & 7), dtile = 4 * (xcd & 1) + (jj >> 3);
  const char* ap = ws + WS_WDP + ((size_t)(5 * dtile) * 48 + 12 * w) * 1024 + lane * 16;
  const char* bp = ws + WS_HHF + ((size_t)(4 * pt) * 48 + 12 * w) * 1024 + lane * 16;
  constexpr int DEPTH = 5;
  bf16x8 A[12][5], B[12][4];
  auto ld = [&](int s) {
#pragma unroll
    for (int j = 0; j < 4; ++j) B[s][j] = *(const bf16x8*)(bp + (size_t)j * 48 * 1024 + s * 1024);
#pragma unroll
    for (int i = 0; i < 5; ++i) A[s][i] = *(const bf16x8*)(ap + (size_t)i * 48 * 1024 + s * 1024);
  };
#pragma unroll
  for (int s = 0; s < DEPTH; ++s) ld(s);
  u32x4 res[3];
  const u16* hT = (const u16*)(ws + WS_HT);
#pragma unroll
  for (int j = 0; j < 3; ++j) {
    int id = tid + 256 * j; id = id < 640 ? id : 639;
    res[j] = *(const u32x4*)(hT + (size_t)(80 * dtile + (id >> 3)) * S_ + 64 * pt + 8 * (id & 7));
  }
  SCHED0();
  f32x4 acc[5][4];
#pragma unroll
  for (int i = 0; i < 5; ++i)
#pragma unroll
    for (int j = 0; j < 4; ++j) acc[i][j] = (f32x4){0.f, 0.f, 0.f, 0.f};
#pragma unroll
  for (int s = 0; s < 12; ++s) {
    if (s + DEPTH < 12) ld(s + DEPTH);
    SCHED0();
#pragma unroll
    for (int i = 0; i < 5; ++i)
#pragma unroll
      for (int j = 0; j < 4; ++j) acc[i][j] = __builtin_amdgcn_mfma_f32_16x16x32_bf16(A[s][i], B[s][j], acc[i][j], 0, 0, 0);
    SCHED0();
  }
  f32x4 own[5];
#pragma unroll
  for (int j = 0; j < 4; ++j) {
    if (j == w) {
#pragma unroll
      for (int i = 0; i < 5; ++i) own[i] = acc[i][j];
    } else {
      const int si = w < j ? w : w - 1;
#pragma unroll
      for (int i = 0; i < 5; ++i) *(LDS_AS f32x4*)(sm + ((j * 3 + si) * 5 + i) * 1024 + lane * 16) = acc[i][j];
    }
  }
#pragma unroll
  for (int j = 0; j < 3; ++j) {
    const int id = tid + 256 * j;
    if (id < 640) {
      const int row = id >> 3, ch = id & 7;
      *(LDS_AS u32x4*)(sm + 61440 + row * 128 + 32 * ((ch >> 1) ^ ((row >> 1) & 3)) + 16 * (ch & 1)) = res[j];
    }
  }
  __syncthreads();
  const int g = lane >> 4, qq = (lane & 15) >> 2, pp = lane & 3;
  const int P = 64 * pt + 16 * w + (lane & 15);
  const int srow = (P & ~15) | (((P >> 2) & 3) << 1) | (P & 1) | (((P >> 1) & 1) << 3);
#pragma unroll
  for (int i = 0; i < 5; ++i) {
    f32x4 t = own[i];
#pragma unroll
    for (int si = 0; si < 3; ++si) t += *(LDS_AS f32x4*)(sm + ((w * 3 + si) * 5 + i) * 1024 + lane * 16);
    const int row = 16 * i + 4 * g + qq;
    const u32x2 hv = tr_read(sm + 61440 + row * 128 + 32 * (w ^ ((row >> 1) & 3)) + 8 * pp);
    u32x2 o = {pack2(bflo(hv.x) + rbf(t.x), bfhi(hv.x) + rbf(t.y)), pack2(bflo(hv.y) + rbf(t.z), bfhi(hv.y) + rbf(t.w))};
    *(u32x2*)(out + (size_t)srow * D_ + 80 * dtile + 16 * i + 4 * g) = o;
  }
}

extern "C" void kernel_launch(void* const* d_in, const int* in_sizes, int n_in, void* d_out, int out_size, void* d_ws,
                              size_t ws_size, hipStream_t stream) {
  const u16* q = (const u16*)d_in[0];
  const u16* k = (const u16*)d_in[1];
  const u16* v = (const u16*)d_in[2];
  const u16* shk = (const u16*)d_in[3];
  const u16* ssk = (const u16*)d_in[4];
  const u16* ssD = (const u16*)d_in[5];
  const u16* wg = (const u16*)d_in[7];
  const u16* wu = (const u16*)d_in[8];
  const u16* wd = (const u16*)d_in[9];
  char* ws = (char*)d_ws;
  (void)hipFuncSetAttribute((const void*)k1_conv, hipFuncAttributeMaxDynamicSharedMemorySize, K1_LDS);
  (void)hipFuncSetAttribute((const void*)k2_gemm1, hipFuncAttributeMaxDynamicSharedMemorySize, K2_LDS);
  (void)hipFuncSetAttribute((const void*)k3_gemm2, hipFuncAttributeMaxDynamicSharedMemorySize, K3_LDS);
  k0_prep<<<448, 256, 0, stream>>>(q, k, v, shk, ws);
  k1_conv<<<256, 256, K1_LDS, stream>>>(ssk, ssD, wg, wu, wd, ws);
  k2_gemm1<<<256, 512, K2_LDS, stream>>>(wg, wu, ws);
  k3_gemm2<<<256, 256, K3_LDS, stream>>>(ws, (u16*)d_out);
}
```

```cpp
#include <hip/hip_runtime.h>
#include <utility>

typedef unsigned short u16;
typedef unsigned int u32;
typedef short s16x4 __attribute__((ext_vector_type(4)));
typedef __bf16 bf16x2 __attribute__((ext_vector_type(2)));
typedef float f32x2 __attribute__((ext_vector_type(2)));
typedef float f32x4 __attribute__((ext_vector_type(4)));
typedef u32 u32x2 __attribute__((ext_vector_type(2)));
typedef u32 u32x4 __attribute__((ext_vector_type(4)));
#define LDS_AS __attribute__((address_space(3)))
#define GLB_AS __attribute__((address_space(1)))
#define CST_AS __attribute__((address_space(4)))

constexpr int S_ = 2048, D_ = 640, F_ = 1536;

constexpr u32 WS_XP = 0;
constexpr u32 WS_KVR = WS_XP + D_ * S_ * 2;
constexpr u32 WS_ST1 = WS_KVR + D_ * S_ * 2;
constexpr u32 WS_HT = WS_ST1 + 16384;
constexpr u32 WS_CS = WS_HT + D_ * S_ * 2;
constexpr u32 WS_WP1 = WS_CS + 32768;
constexpr u32 WS_WP2 = WS_WP1 + 2 * D_ * F_ * 2;
constexpr u32 WS_HH = WS_WP2 + D_ * F_ * 2;
constexpr u32 WS_END = WS_HH + S_ * F_ * 2;

__device__ __forceinline__ float bflo(u32 x) { return __uint_as_float(x << 16); }
__device__ __forceinline__ float bfhi(u32 x) { return __uint_as_float(x & 0xffff0000u); }
__device__ __forceinline__ u32 pack2(float lo, float hi) {
  bf16x2 v; v.x = (__bf16)lo; v.y = (__bf16)hi;
  return __builtin_bit_cast(u32, v);
}
__device__ __forceinline__ float rbf(float f) { __bf16 b = (__bf16)f; return (float)b; }

__device__ __forceinline__ void glds16(const void* g, LDS_AS char* l) {
  __builtin_amdgcn_global_load_lds((const GLB_AS void*)g, (LDS_AS void*)l, 16, 0, 0);
}
__device__ __forceinline__ u32x2 tr_read(LDS_AS char* p) {
  s16x4 v = __builtin_amdgcn_ds_read_tr16_b64_v4i16((LDS_AS s16x4*)p);
  return __builtin_bit_cast(u32x2, v);
}
__device__ __forceinline__ u32x4 mk4(u32x2 a, u32x2 b) { return (u32x4){a.x, a.y, b.x, b.y}; }
__device__ __forceinline__ u32x4 mkrsrc(const void* p, u32 nbytes) {
  const unsigned long long a = (unsigned long long)p;
  return (u32x4){(u32)a, (u32)(a >> 32) & 0xffffu, nbytes, 0x00020000u};
}
#define WAIT_VM(n) asm volatile("s_waitcnt vmcnt(%0)" ::"n"(n) : "memory")
#define WAITB_VM(n) __builtin_amdgcn_s_waitcnt(((n) & 15) | 0xf70 | (((n) >> 4) << 14))
#define WAIT_LGKM0() asm volatile("s_waitcnt lgkmcnt(0)" ::: "memory")
#define SBAR() asm volatile("s_barrier" ::: "memory")
#define SCHED0() __builtin_amdgcn_sched_barrier(0)
#define PIN(x) asm volatile("" : "+v"(x))
#define USE(x) asm volatile("" ::"v"(x))
__device__ __forceinline__ void dma1(u32 lds, u32x4 rs, u32 voff, u32 soff) {
  asm volatile("s_mov_b32 m0, %0\n\ts_nop 0\n\tbuffer_load_dwordx4 %1, %2, %3 offen lds" ::"s"(lds), "v"(voff), "s"(rs), "s"(soff) : "memory");
}
__device__ __forceinline__ void dma2(u32 lds, u32x4 rs, u32 voff, u32 soff) {
  asm volatile("s_mov_b32 m0, %0\n\ts_nop 0\n\tbuffer_load_dwordx4 %1, %2, %3 offen lds\n\tbuffer_load_dwordx4 %1, %2, %3 offen offset:1024 lds"
               ::"s"(lds), "v"(voff), "s"(rs), "s"(soff) : "memory");
}
__device__ __forceinline__ void dma3(u32 lds, u32x4 rs, u32 voff, u32 soff) {
  asm volatile("s_mov_b32 m0, %0\n\ts_nop 0\n\tbuffer_load_dwordx4 %1, %2, %3 offen lds\n\tbuffer_load_dwordx4 %1, %2, %3 offen offset:1024 lds\n\t"
               "buffer_load_dwordx4 %1, %2, %3 offen offset:2048 lds"
               ::"s"(lds), "v"(voff), "s"(rs), "s"(soff) : "memory");
}
__device__ __forceinline__ void dma4(u32 lds, u32x4 rs, u32 voff, u32 soff) {
  asm volatile("s_mov_b32 m0, %0\n\ts_nop 0\n\tbuffer_load_dwordx4 %1, %2, %3 offen lds\n\tbuffer_load_dwordx4 %1, %2, %3 offen offset:1024 lds\n\t"
               "buffer_load_dwordx4 %1, %2, %3 offen offset:2048 lds\n\tbuffer_load_dwordx4 %1, %2, %3 offen offset:3072 lds"
               ::"s"(lds), "v"(voff), "s"(rs), "s"(soff) : "memory");
}
template <int IMM> __device__ __forceinline__ void ld_a(u32x4& d, u32x4 rs, u32 voff, u32 soff) {
  asm volatile("buffer_load_dwordx4 %0, %1, %2, %3 offen offset:%4" : "=a"(d) : "v"(voff), "s"(rs), "s"(soff), "n"(IMM) : "memory");
}
template <int IMM> __device__ __forceinline__ void ld_v(u32x4& d, u32x4 rs, u32 voff, u32 soff) {
  asm volatile("buffer_load_dwordx4 %0, %1, %2, %3 offen offset:%4" : "=v"(d) : "v"(voff), "s"(rs), "s"(soff), "n"(IMM) : "memory");
}
__device__ __forceinline__ void mfma_vv(f32x4& c, const u32x4& a, const u32x4& b) {
  asm volatile("v_mfma_f32_16x16x32_bf16 %0, %1, %2, %0" : "+v"(c) : "v"(a), "v"(b) : "memory");
}
__device__ __forceinline__ void mfma_vv0(f32x4& c, const u32x4& a, const u32x4& b) {
  asm volatile("v_mfma_f32_16x16x32_bf16 %0, %1, %2, 0" : "=&v"(c) : "v"(a), "v"(b) : "memory");
}
__device__ __forceinline__ void mfma_av(f32x4& c, const u32x4& a, const u32x4& b) {
  asm volatile("v_mfma_f32_16x16x32_bf16 %0, %1, %2, %0" : "+v"(c) : "a"(a), "v"(b) : "memory");
}
__device__ __forceinline__ void mfma_av0(f32x4& c, const u32x4& a, const u32x4& b) {
  asm volatile("v_mfma_f32_16x16x32_bf16 %0, %1, %2, 0" : "=&v"(c) : "a"(a), "v"(b) : "memory");
}
template <class Fn, int... I> __device__ __forceinline__ void sfor_impl(Fn&& f, std::integer_sequence<int, I...>) {
  (f(std::integral_constant<int, I>{}), ...);
}
template <int N, class Fn> __device__ __forceinline__ void sfor(Fn&& f) { sfor_impl(f, std::make_integer_sequence<int, N>{}); }

#define PROF_DECL
#define STAMP(i)
#define RSTAMP(i)
#define PROF_END(kid)

__global__ __launch_bounds__(256) void k0_prep(const u16* __restrict__ q, const u16* __restrict__ k,
                                               const u16* __restrict__ v, const u16* __restrict__ shk,
                                               char* __restrict__ ws) {
  __shared__ __attribute__((aligned(16))) char smraw[8192 + 8192 + 9216];
  LDS_AS char* sm = (LDS_AS char*)smraw;
  const int bid = blockIdx.x, tid = threadIdx.x, lane = tid & 63;
  const int w = __builtin_amdgcn_readfirstlane(tid >> 6);
  PROF_DECL;
  if (bid < 320) {
    const int rb = bid / 10, cb = bid % 10, s0 = rb * 64, c0 = cb * 64;
    const int xs0 = 512 * (rb >> 3) + 32 * (rb & 7);
    const int i = lane & 15, g = lane >> 4, qq = (lane & 15) >> 2, pp = lane & 3;
    const int c = c0 + 16 * w + i;
    const u32x2 shv = *(const u32x2*)(shk + c * 4);
    {
      const int r8 = lane >> 3, unit = (lane >> 1) & 3, half = lane & 1;
#pragma unroll
      for (int j = 0; j < 3; ++j) {
        const int it = w + 4 * j;
        if (it < 9) {
          const int row = it * 8 + r8;
          const int f = ((row >> 1) & 1) | (((row >> 3) & 1) << 1);
          int sr = s0 - 8 + row; sr = sr < 0 ? 0 : sr;
          glds16(k + (size_t)sr * D_ + c0 + 16 * (unit ^ f) + 8 * half, sm + 16384 + it * 1024);
        }
      }
#pragma unroll
      for (int j = 0; j < 2; ++j) {
        const int it = w + 4 * j, row = it * 8 + r8;
        const int f = ((row >> 1) & 1) | (((row >> 3) & 1) << 1);
        glds16(v + (size_t)(s0 + row) * D_ + c0 + 16 * (unit ^ f) + 8 * half, sm + 8192 + it * 1024);
      }
#pragma unroll
      for (int j = 0; j < 2; ++j) {
        const int it = w + 4 * j, row = it * 8 + r8;
        const int f = ((row >> 1) & 1) | (((row >> 4) & 1) << 1);
        glds16(q + (size_t)(xs0 + 256 * (row >> 5) + (row & 31)) * D_ + c0 + 16 * (unit ^ f) + 8 * half, sm + it * 1024);
      }
    }
    STAMP(1);
    WAIT_VM(0);
    STAMP(2);
    __syncthreads();
    STAMP(3);
    const float w0 = bflo(shv.x), w1 = bfhi(shv.x), w2 = bflo(shv.y), w3 = bfhi(shv.y);
    u16* xP = (u16*)(ws + WS_XP);
    u16* kvR = (u16*)(ws + WS_KVR);
#pragma unroll
    for (int sbi = 0; sbi < 2; ++sbi) {
      const int sb = sbi * 32;
      u32x2 xa[2];
#pragma unroll
      for (int t = 0; t < 2; ++t) {
        const int row = 32 * t + 16 * (qq >> 1) + 2 * (4 * sbi + g) + (qq & 1);
        const int f = ((row >> 1) & 1) | (((row >> 4) & 1) << 1);
        xa[t] = tr_read(sm + row * 128 + 32 * (w ^ f) + 8 * pp);
      }
      *(u32x4*)(xP + (size_t)c * S_ + s0 + sb + 8 * g) = mk4(xa[0], xa[1]);
      u32x2 va[2], ka[3];
#pragma unroll
      for (int t = 0; t < 2; ++t) {
        const int row = sb + 8 * g + 4 * t + qq;
        const int f = ((row >> 1) & 1) | (((row >> 3) & 1) << 1);
        va[t] = tr_read(sm + 8192 + row * 128 + 32 * (w ^ f) + 8 * pp);
      }
#pragma unroll
      for (int t = 0; t < 3; ++t) {
        const int row = sb + 8 * g + 4 + 4 * t + qq;
        const int f = ((row >> 1) & 1) | (((row >> 3) & 1) << 1);
        ka[t] = tr_read(sm + 16384 + row * 128 + 32 * (w ^ f) + 8 * pp);
      }
      if (s0 + sb + 8 * g == 0) { ka[0].x = 0u; ka[0].y = 0u; }
      float kf[12], vf[8], o[8];
#pragma unroll
      for (int t = 0; t < 3; ++t) {
        kf[4 * t] = bflo(ka[t].x); kf[4 * t + 1] = bfhi(ka[t].x); kf[4 * t + 2] = bflo(ka[t].y); kf[4 * t + 3] = bfhi(ka[t].y);
      }
#pragma unroll
      for (int t = 0; t < 2; ++t) {
        vf[4 * t] = bflo(va[t].x); vf[4 * t + 1] = bfhi(va[t].x); vf[4 * t + 2] = bflo(va[t].y); vf[4 * t + 3] = bfhi(va[t].y);
      }
#pragma unroll
      for (int e = 0; e < 8; ++e) {
        float a = kf[e + 1] * w3;
        a = fmaf(kf[e + 2], w2, a);
        a = fmaf(kf[e + 3], w1, a);
        a = fmaf(kf[e + 4], w0, a);
        o[e] = a * vf[e];
      }
      u32x4 ov = {pack2(o[7], o[6]), pack2(o[5], o[4]), pack2(o[3], o[2]), pack2(o[1], o[0])};
      *(u32x4*)(kvR + (size_t)c * S_ + 2040 - (s0 + sb + 8 * g)) = ov;
    }
  } else {
    const int s = (bid - 320) * 16 + (tid >> 4), l16 = tid & 15;
    u32x4 d[5];
#pragma unroll
    for (int j = 0; j < 5; ++j) d[j] = *(const u32x4*)(q + (size_t)s * D_ + (j * 16 + l16) * 8);
    float sum = 0.f;
#pragma unroll
    for (int j = 0; j < 5; ++j)
#pragma unroll
      for (int e = 0; e < 4; ++e) sum += bflo(d[j][e]) + bfhi(d[j][e]);
#pragma unroll
    for (int o = 1; o < 16; o <<= 1) sum += __shfl_xor(sum, o);
    const float mean = sum * (1.f / D_);
    float sq = 0.f;
#pragma unroll
    for (int j = 0; j < 5; ++j)
#pragma unroll
      for (int e = 0; e < 4; ++e) {
        float a = bflo(d[j][e]) - mean, b = bfhi(d[j][e]) - mean;
        sq = fmaf(a, a, sq); sq = fmaf(b, b, sq);
      }
#pragma unroll
    for (int o = 1; o < 16; o <<= 1) sq += __shfl_xor(sq, o);
    if (l16 == 0) {
      const float mb = rbf(mean);
      const float den = rbf(sqrtf(rbf(rbf(sq * (1.f / D_)) + 1e-5f)));
      const float r = 1.f / den;
      const int m = s >> 9, nb = (s >> 8) & 1, qd = 2 * ((s >> 7) & 1) + ((s >> 5) & 1);
      const int a = 8 * ((s >> 6) & 1) + ((s >> 1) & 7), reg = 2 * ((s >> 4) & 1) + (s & 1);
      float* st = (float*)(ws + WS_ST1);
      st[(((m * 2 + 0) * 2 + nb) * 64 + a + 16 * qd) * 4 + reg] = r;
      st[(((m * 2 + 1) * 2 + nb) * 64 + a + 16 * qd) * 4 + reg] = -mb * r;
    }
  }
  STAMP(4);
  PROF_END(0);
}

constexpr int K1_FB = 4480, K1_GA = 8960, K1_GB = 13248, K1_WAVE = 17536, K1_ST = 4 * K1_WAVE;
constexpr int K1_LDS = K1_ST + 16384;
struct PairTab { int kk[36], m[36]; };
constexpr PairTab make_pairs() {
  PairTab t{}; int n = 0;
  for (int d = 0; d < 8; ++d) {
    for (int e = 0; e < d; ++e) {
      if (d + e <= 7) { t.kk[n] = e; t.m[n] = d; ++n; }
    }
    for (int e = 0; e <= d; ++e) {
      if (d + e <= 7) { t.kk[n] = d; t.m[n] = e; ++n; }
    }
  }
  return t;
}
constexpr PairTab PT = make_pairs();
constexpr bool first_in_phase(int j) {
  for (int i = 0; i < j; ++i) if (PT.kk[i] + PT.m[i] == PT.kk[j] + PT.m[j]) return false;
  return true;
}

__global__ __launch_bounds__(256, 1) void k1_conv(const u16* __restrict__ ssk, const u16* __restrict__ ssD,
                                                  const u16* __restrict__ wg, const u16* __restrict__ wu,
                                                  const u16* __restrict__ wd, char* __restrict__ ws) {
  extern __shared__ __attribute__((aligned(16))) char smraw[];
  LDS_AS char* sm = (LDS_AS char*)smraw;
  const u32 smb = (u32)(size_t)sm;
  const int bid = blockIdx.x, tid = threadIdx.x, lane = tid & 63;
  const int w = __builtin_amdgcn_readfirstlane(tid >> 6);
  PROF_DECL;
  if (bid < 160) {
    const int c = bid * 4 + w;
    LDS_AS char* wb = sm + w * K1_WAVE;
    const u32 wba = smb + w * K1_WAVE;
    const u16* krow = ssk + (size_t)c * S_;
    const u32x4 rk = mkrsrc(krow, 4096), rg = mkrsrc(ws + WS_KVR + (size_t)c * 4096, 4096), rw = mkrsrc(ws, WS_END);
    const u32 l16 = lane * 16;
    const u32x4 k8 = *(const CST_AS u32x4*)krow;
    const u32 dw = *(const CST_AS u32*)(ssD + (c & ~1));
    dma4(wba + K1_GB, rg, l16, 0);
    dma4(wba + K1_FB + 352, rk, l16, 0);
    dma4(wba + K1_GA, rg, l16 + 4, 0);
    dma1(wba + 352, rk, lane ? l16 - 2 : 0u, 0);
    dma3(wba + 352 + 1024, rk, l16 + 1022, 0);
    dma4(smb + K1_ST + w * 4096, rw, l16, WS_ST1 + w * 4096);
    const int a = lane & 15, qd = lane >> 4;
    const int sg = 4 * (qd >> 1) + 2 * (a >> 3) + (qd & 1);
    u32x4 xv[4];
    sfor<4>([&](auto M) { ld_v<1024 * M>(xv[M], rw, (u32)(128 * sg + 16 * (a & 7)), WS_XP + c * 4096); });
    {
      int o = l16;
      o += lane >= 22 ? K1_FB - 16 * 22 : 0;
      o += lane >= 44 ? (K1_GA + 4096 - 16 * 44) - (K1_FB - 16 * 22) : 0;
      o += lane >= 54 ? (K1_GB - 16 * 54) - (K1_GA - 16 * 44) : 0;
      *(LDS_AS u32x4*)(wb + o) = (u32x4){0u, 0u, 0u, 0u};
    }
    const u32 k0p = pack2(bflo(k8.x) + ((c & 1) ? bfhi(dw) : bflo(dw)), 0.f) & 0xffffu;
    const u32 d0 = (k8.x & 0xffff0000u) | k0p;
    u32x4 fix = {k0p << 16, __builtin_amdgcn_alignbit(k8.y, k8.x, 16), __builtin_amdgcn_alignbit(k8.z, k8.y, 16), __builtin_amdgcn_alignbit(k8.w, k8.z, 16)};
    u32 fix2 = d0;
    PIN(fix); PIN(fix2);
    LDS_AS char* fb = wb + ((a & 1) ? K1_FB : 0) + 32 * ((a >> 1) & 1) + 64 * ((a >> 2) & 1) + 256 * (a >> 3) + 16 * qd;
    LDS_AS char* gb = wb + ((a & 1) ? K1_GB : K1_GA) + 2 * 1868 - 3584 - 8 * ((a >> 1) & 3) - 128 * (a >> 3) + 16 * qd;
    LDS_AS char* stp = sm + K1_ST + l16;
    PIN(fb); PIN(gb); PIN(stp);
    STAMP(1);
    WAIT_VM(8);
    if (lane == 0) {
      *(LDS_AS u32x4*)(wb + 352) = fix;
      *(LDS_AS u32*)(wb + K1_FB + 352) = fix2;
    }
    STAMP(2);
    f32x4 acc[8];
    u32x4 Ff[2][8];
    u32x2 Gl[2][8], Gh[2][8];
    f32x4 ql[8], xf[8], rr[2], mm[2];
    auto rd = [&](auto SET, auto R, auto J) {
      constexpr int set = SET, r = R, j = J, d = j / 3, t = j % 3;
      if constexpr (t == 0) Gl[set][d] = *(LDS_AS u32x2*)(gb + 3584 - 512 * d + 64 * r);
      else if constexpr (t == 1) Gh[set][d] = *(LDS_AS u32x2*)(gb + 3584 - 512 * d + 64 * r + 8);
      else Ff[set][d] = *(LDS_AS u32x4*)(fb + 512 * d + 64 * r);
    };
    sfor<24>([&](auto J) { rd(std::integral_constant<int, 0>{}, std::integral_constant<int, 0>{}, J); asm volatile("" ::: "memory"); });
    sfor<8>([&](auto R) {
      constexpr int r = R, cur = r & 1;
      sfor<36>([&](auto J) {
        constexpr int j = J, kk = PT.kk[j], m = PT.m[j];
        const u32x4 G = mk4(Gl[cur][m], Gh[cur][m]);
        if constexpr (r == 0 && first_in_phase(j)) mfma_vv0(acc[kk + m], Ff[cur][kk], G);
        else mfma_vv(acc[kk + m], Ff[cur][kk], G);
        if constexpr (r < 7 && j >= 4 && j < 28) rd(std::integral_constant<int, cur ^ 1>{}, std::integral_constant<int, r + 1>{}, std::integral_constant<int, j - 4>{});
        if constexpr (r >= 2 && r < 6) {
          constexpr int mg = r - 2;
          if constexpr (j == 0 && mg == 0) asm volatile("s_waitcnt vmcnt(0)\n\ts_barrier" : "+v"(xv[0]), "+v"(xv[1]), "+v"(xv[2]), "+v"(xv[3])::"memory");
          if constexpr (j == 0) rr[0] = *(LDS_AS f32x4*)(stp + (mg * 4 + 0) * 1024);
          if constexpr (j == 1) mm[0] = *(LDS_AS f32x4*)(stp + (mg * 4 + 2) * 1024);
          if constexpr (j == 2) rr[1] = *(LDS_AS f32x4*)(stp + (mg * 4 + 1) * 1024);
          if constexpr (j == 3) mm[1] = *(LDS_AS f32x4*)(stp + (mg * 4 + 3) * 1024);
          if constexpr (j >= 28) {
            constexpr int e = j - 28, nb = e >> 2, reg = e & 3;
            const u32 xw = xv[mg][2 * nb + (reg >> 1)];
            float x = (reg & 1) ? bfhi(xw) : bflo(xw);
            float t = fmaf(x, rr[nb][reg], mm[nb][reg]);
            PIN(x); PIN(t);
            xf[2 * mg + nb][reg] = x; ql[2 * mg + nb][reg] = t;
          }
        }
      });
    });
    asm volatile("s_nop 7" : "+v"(acc[0]), "+v"(acc[1]), "+v"(acc[2]), "+v"(acc[3]), "+v"(acc[4]), "+v"(acc[5]), "+v"(acc[6]), "+v"(acc[7]));
    STAMP(3);
    char* hp = ws + WS_HT + ((size_t)(2 * sg + ((a >> 2) & 1)) * D_ + c) * 64 + 16 * ((a & 3) ^ (2 * (bid & 1)));
#pragma unroll
    for (int m = 0; m < 4; ++m) {
      const f32x4 h0 = ql[2 * m] * acc[2 * m] + xf[2 * m];
      const f32x4 h1 = ql[2 * m + 1] * acc[2 * m + 1] + xf[2 * m + 1];
      *(u32x4*)(hp + (size_t)m * 16 * D_ * 64) = (u32x4){pack2(h0.x, h0.y), pack2(h0.z, h0.w), pack2(h1.x, h1.y), pack2(h1.z, h1.w)};
    }
  } else if (bid < 208) {
    const int jb = bid - 160, cg = jb >> 1, kh = jb & 1;
    {
      const int r8 = lane >> 3, ch = lane & 7;
#pragma unroll
      for (int it = 0; it < 20; ++it) {
        const int I = w * 20 + it, img = I / 40, row = (I % 40) * 8 + r8;
        const u16* src = (img ? wu : wg) + (size_t)(kh * 320 + row) * F_ + cg * 64 + 8 * (ch ^ (((row >> 1) & 3) | (img << 2)));
        glds16(src, sm + I * 1024);
      }
    }
    WAIT_VM(0);
    __syncthreads();
    const int g = lane >> 4, qq = (lane & 15) >> 2, pp = lane & 3, img = pp & 1;
    const int i = lane & 15, ml = (i & 9) | ((i & 2) << 1) | ((i & 4) >> 1);
    const u32x4 ones = {0x3f803f80u, 0x3f803f80u, 0x3f803f80u, 0x3f803f80u};
#pragma unroll
    for (int tt = 0; tt < 2; ++tt) {
      const int tl = 2 * w + tt, tau = cg * 8 + tl, slot = tau / 3, ii = tau - slot * 3;
      f32x4 cs = {0.f, 0.f, 0.f, 0.f};
#pragma unroll
      for (int ks = 0; ks < 10; ++ks) {
        u32x2 a[2];
#pragma unroll
        for (int t = 0; t < 2; ++t) {
          const int row = 32 * ks + 16 * t + 4 * g + qq;
          a[t] = tr_read(sm + img * 40960 + row * 128 + 16 * (tl ^ (((row >> 1) & 3) | (img << 2))) + 8 * (pp >> 1));
        }
        const u32x4 fr = mk4(a[0], a[1]);
        typedef __bf16 bf16x8 __attribute__((ext_vector_type(8)));
        cs = __builtin_amdgcn_mfma_f32_16x16x32_bf16(__builtin_bit_cast(bf16x8, fr), __builtin_bit_cast(bf16x8, ones), cs, 0, 0, 0);
        const int kstep = (kh ^ ((slot >> 1) & 1)) * 10 + ks;
        *(u32x4*)(ws + WS_WP1 + ((size_t)((slot * 20 + kstep) * 3 + ii) * 64 + 16 * g + ml) * 16) = fr;
      }
      if (i == 0) {
        float* csp = (float*)(ws + WS_CS) + (kh * 192 + tau) * 16 + 8 * (g >> 1) + 2 * (g & 1);
        *(f32x2*)(csp) = (f32x2){cs.x, cs.y};
        *(f32x2*)(csp + 4) = (f32x2){cs.z, cs.w};
      }
    }
  } else {
    const int ks = bid - 208;
    const u16* src = wd + (size_t)ks * 32 * D_;
#pragma unroll
    for (int it = 0; it < 10; ++it) {
      const int I = w * 10 + it, id = I * 64 + lane, row = id / 80, ch = id - row * 80;
      glds16(src + row * D_ + 8 * ch, sm + I * 1024);
    }
    WAIT_VM(0);
    __syncthreads();
    const int g = lane >> 4, qq = (lane & 15) >> 2, pp = lane & 3;
#pragma unroll
    for (int it = 0; it < 10; ++it) {
      const int dtile = 2 * w + it / 5, ii = it % 5;
      const int dl = 80 * dtile + (ii < 4 ? 32 * (ii >> 1) + 8 * pp + 4 * (ii & 1) : 64 + 4 * pp);
      u32x2 a[2];
#pragma unroll
      for (int t = 0; t < 2; ++t) a[t] = tr_read(sm + (8 * g + 4 * t + qq) * 1280 + 2 * dl);
      *(u32x4*)(ws + WS_WP2 + ((size_t)((dtile * 48 + ks) * 5 + ii) * 64 + lane) * 16) = mk4(a[0], a[1]);
    }
  }
  STAMP(5);
  PROF_END(1);
}

constexpr int K2_LDS = 163840;
template <int N> using IC = std::integral_constant<int, N>;

struct K2Q {
  int idxH[5], idxW[20], idxQ1[5], idxQ2, idxQ3, issued[81];
};
constexpr K2Q make_k2q() {
  K2Q t{}; int n = 6;
  t.idxH[0] = n + 1; n += 2; t.idxH[1] = n + 1; n += 2;
  for (int k = 0; k < 12; ++k) {
    t.idxW[k] = n + 2; n += 3;
    if (k == 1 || k == 3 || k == 5) { t.idxH[k / 2 + 2] = n + 1; n += 2; }
  }
  for (int T = 0; T <= 80; ++T) {
    t.issued[T] = n;
    if (T < 8) { t.idxW[12 + T] = n + 2; n += 3; }
    else if (T < 13) { t.idxQ1[T - 8] = n + 1; n += 2; }
    else if (T < 15) n += 4;
    else if (T == 15) { n += 2; t.idxQ2 = n - 1; }
    else if (T < 18) n += 4;
    else if (T == 18) { n += 2; t.idxQ3 = n - 1; }
  }
  return t;
}
constexpr K2Q KQ = make_k2q();

constexpr int OP_EXT = 126, OP_MATH = 130, OP_TS = 160, OP_END = 175;
constexpr int op_cost(int id) {
  if (id < OP_EXT) { const int st = id % 21; return (st == 8 || st == 9 || st == 14 || st == 15) ? 2 : 1; }
  if (id >= OP_MATH && id < OP_TS) { const int m = id - OP_MATH; return (m == 12 || m == 15) ? 2 : 1; }
  return 1;
}
struct K2Sched { int n[160]; int op[160][8]; bool ok; };
constexpr K2Sched make_sched(bool vals, bool ts, int boost) {
  K2Sched t{};
  int cap[160] = {};
  int cnt = 0;
  for (int s = 0; s < 20; ++s)
    for (int x = 0; x < (s < 10 ? 8 : 6); ++x) {
      int c = (x >= 2 && x < 6) ? 1 : 2;
      if (x == (s < 10 ? 7 : 1)) c -= 1;
      ++cnt;
      if (boost > 0 && cnt % boost == 0) c += 1;
      cap[8 * s + x] = c;
    }
  auto place = [&](int first, int last, int gap) {
    int carry = 0;
    for (int id = first; id < last; ++id) {
      while (gap < 160 && cap[gap] + carry <= 0) { carry += cap[gap]; cap[gap] = 0; if (carry > 0) carry = 0; ++gap; }
      if (gap >= 160) return false;
      cap[gap] += carry; carry = 0;
      t.op[gap][t.n[gap]++] = id;
      cap[gap] -= op_cost(id);
      if (cap[gap] < 0) { carry = cap[gap]; cap[gap] = 0; ++gap; }
    }
    return true;
  };
  bool ok = place(OP_EXT, OP_EXT + 4, 8 * 10 + 3);
  ok = ok && place(OP_MATH, OP_MATH + 29, 8 * 15);
  if (vals) ok = ok && place(0, 126, 0);
  if (ts) ok = ok && place(OP_TS, OP_END, 8 * 14);
  t.ok = ok;
  return t;
}
constexpr K2Sched find_sched(bool vals, bool ts) {
  for (int b = 0; b < 40; ++b) {
    const int boost = b == 0 ? 0 : 41 - b;
    K2Sched t = make_sched(vals, ts, boost);
    if (t.ok) return t;
  }
  return K2Sched{};
}
constexpr K2Sched KS0 = find_sched(false, false), KS1 = find_sched(true, false), KS2 = find_sched(true, true);
static_assert(KS0.ok && KS1.ok && KS2.ok, "schedule");
template <int Q> constexpr const K2Sched& ksched() { if constexpr (Q == 0) return KS0; else if constexpr (Q == 2) return KS2; else return KS1; }

__global__ __launch_bounds__(256, 1) void k2_gemm1(char* __restrict__ ws) {
  extern __shared__ __attribute__((aligned(16))) char smraw[];
  LDS_AS char* sm = (LDS_AS char*)smraw;
  const u32 smb = (u32)(size_t)sm;
  const int bid = blockIdx.x, tid = threadIdx.x, lane = tid & 63;
  const int w = __builtin_amdgcn_readfirstlane(tid >> 6);
  PROF_DECL;
  const int xcd = bid & 7, jb = bid >> 3;
  const int pm = 8 * (xcd & 1) + (jb & 7), fn = 4 * (xcd >> 1) + (jb >> 3);
  const int hi = w >> 1, lo = w & 1;
  const u32x4 rw = mkrsrc(ws, WS_END);
  const u32 l16 = lane * 16;
  const int n = lane & 15, g = lane >> 4, qq = n >> 2, pp = lane & 3;
  const u32 hsrc = WS_HT + pm * 163840, wsrc = WS_WP1 + (4 * fn + w) * 61440;
  u32x4 W[20][3], csr[2][3];
  sfor<2>([&](auto K) { sfor<3>([&](auto I) { ld_v<64 * I>(csr[K][I], rw, (u32)(16 * g), WS_CS + (K * 192 + 12 * fn + 3 * w) * 64); }); });
  auto ldW = [&](auto K) { sfor<3>([&](auto I) { ld_a<1024 * I>(W[K][I], rw, l16, wsrc + K * 3072); }); };
  auto ldH = [&](int q, int u) { const u32 o = q * 40960 + (2 * u + lo + 10 * hi) * 2048; dma2(smb + o, rw, l16, hsrc + o); };
  ldH(0, 0); ldH(0, 1);
  sfor<12>([&](auto K) { ldW(K); if constexpr (K == 1 || K == 3 || K == 5) ldH(0, K / 2 + 2); });
  STAMP(1);
  LDS_AS char* bp[2][2];
#pragma unroll
  for (int jj = 0; jj < 2; ++jj) {
    bp[0][jj] = sm + 256 * g + 64 * qq + 32 * (jj ^ lo ^ (g & 1)) + 8 * pp + 20480 * hi;
    bp[1][jj] = sm + 256 * g + 64 * qq + 32 * (jj ^ lo ^ (g & 1)) + 8 * pp + 20480 - 20480 * hi;
  }
  LDS_AS char* stw = sm + (g == qq ? lo * 256 + n * 16 + hi * 8 : 512 + lane * 8);
  LDS_AS char* str = sm + ((lane >> 5) ^ lo) * 256 + n * 16;
  char* hhp = ws + WS_HH + (size_t)(8 * pm + (g ^ lo)) * 49152 + (12 * fn + 3 * w) * 256 + n * 16;
  u32x4 ones = {0x3f803f80u, 0x3f803f80u, 0x3f803f80u, 0x3f803f80u};
  PIN(ones);
  u32x2 Bl[3][2], Bh[3][2];
  f32x4 acc[2][6], sg, so, cs[3], sv;
  float cst[2][2][4];
  u32 pk[2][3][4];
  float G0, G1, U0, U1, T0, T1, m0, m1, m2, m3, mc[4];
  u32 mi;
  auto rd = [&](auto TT, auto J) {
    constexpr int T = TT, j = J, q = T / 20, s = T % 20, jj = 1 - (j >> 1), t = j & 1;
    const u32x2 v = tr_read(bp[s / 10][jj] + (q & 1) * 40960 + (s % 10) * 2048 + t * 1024);
    if constexpr (t == 0) Bl[T % 3][jj] = v; else Bh[T % 3][jj] = v;
  };
  constexpr float k0c = -2.f * 0.7978845608028654f * 1.4426950408889634f;
  auto op = [&](auto Q, auto ID) {
    constexpr int q = Q, id = ID;
    if constexpr (id < OP_EXT) {
      constexpr int t = id / 21, st = id % 21, i = t >> 1, jj = t & 1, set = (q - 1) & 1;
      const f32x4 a = acc[set][t];
      const float nmu = cst[set][jj][0], K0 = cst[set][jj][1], K1 = cst[set][jj][2], ir2 = cst[set][jj][3];
      if constexpr (st == 0) { G0 = fmaf(cs[i].x, nmu, a.x); PIN(G0); }
      if constexpr (st == 1) { G1 = fmaf(cs[i].y, nmu, a.y); PIN(G1); }
      if constexpr (st == 2) { T0 = G0 * G0; PIN(T0); }
      if constexpr (st == 3) { T1 = G1 * G1; PIN(T1); }
      if constexpr (st == 4) { T0 = fmaf(T0, K1, K0); PIN(T0); }
      if constexpr (st == 5) { T1 = fmaf(T1, K1, K0); PIN(T1); }
      if constexpr (st == 6) { T0 = T0 * G0; PIN(T0); }
      if constexpr (st == 7) { T1 = T1 * G1; PIN(T1); }
      if constexpr (st == 8) { T0 = __builtin_amdgcn_exp2f(T0); PIN(T0); }
      if constexpr (st == 9) { T1 = __builtin_amdgcn_exp2f(T1); PIN(T1); }
      if constexpr (st == 10) { U0 = fmaf(cs[i].z, nmu, a.z); PIN(U0); }
      if constexpr (st == 11) { U1 = fmaf(cs[i].w, nmu, a.w); PIN(U1); }
      if constexpr (st == 12) { T0 = fmaf(T0, ir2, ir2); PIN(T0); }
      if constexpr (st == 13) { T1 = fmaf(T1, ir2, ir2); PIN(T1); }
      if constexpr (st == 14) { T0 = __builtin_amdgcn_rcpf(T0); PIN(T0); }
      if constexpr (st == 15) { T1 = __builtin_amdgcn_rcpf(T1); PIN(T1); }
      if constexpr (st == 16) { U0 = U0 * G0; PIN(U0); }
      if constexpr (st == 17) { U1 = U1 * G1; PIN(U1); }
      if constexpr (st == 18) { U0 = U0 * T0; PIN(U0); }
      if constexpr (st == 19) { U1 = U1 * T1; PIN(U1); }
      if constexpr (st == 20) { u32 p = pack2(U0, U1); PIN(p); pk[(q - 1) >> 1][i][2 * set + jj] = p; }
    } else if constexpr (id < OP_MATH) {
      constexpr int e = id - OP_EXT;
      if constexpr (e == 0) asm volatile("v_cndmask_b32_e64 %0, %1, %2, %3" : "=v"(m0) : "v"(sg.x), "v"(sg.y), "s"(0xaaaaaaaaaaaaaaaaull));
      if constexpr (e == 1) asm volatile("v_cndmask_b32_e64 %0, %1, %2, %3" : "=v"(m1) : "v"(sg.z), "v"(sg.w), "s"(0xaaaaaaaaaaaaaaaaull));
      if constexpr (e == 2) asm volatile("v_cndmask_b32_e64 %0, %1, %2, %3" : "=v"(so.y) : "v"(m0), "v"(m1), "s"(0xccccccccccccccccull));
      if constexpr (e == 3) *(LDS_AS f32x2*)(stw + (q == 0 ? 162816 : (q - 1) * 1024)) = (f32x2){so.x, so.y};
    } else if constexpr (id < OP_TS) {
      constexpr int m = id - OP_MATH, set = q & 1;
      if constexpr (m == 0) { m0 = sv.x + sv.z; PIN(m0); }
      if constexpr (m == 1) { m1 = sv.y + sv.w; PIN(m1); }
      if constexpr (m == 2) { m0 = m0 * (-1.f / D_); PIN(m0); }
      if constexpr (m == 3) { m1 = m1 * (1.f / D_); PIN(m1); }
      if constexpr (m == 4) { m1 = fmaf(-m0, m0, m1); PIN(m1); }
      if constexpr (m == 5) { m1 = __builtin_amdgcn_fmed3f(m1, 0.f, 3.0e38f); PIN(m1); }
      if constexpr (m == 6) { mi = pack2(m0, m1); PIN(mi); }
      if constexpr (m == 7) { m0 = bflo(mi); PIN(m0); }
      if constexpr (m == 8) { m1 = bfhi(mi); PIN(m1); }
      if constexpr (m == 9) { m1 = m1 + 1e-5f; PIN(m1); }
      if constexpr (m == 10) { mi = pack2(m1, m1); PIN(mi); }
      if constexpr (m == 11) { m1 = bflo(mi); PIN(m1); }
      if constexpr (m == 12) { m1 = __builtin_amdgcn_sqrtf(m1); PIN(m1); }
      if constexpr (m == 13) { mi = pack2(m1, m1); PIN(mi); }
      if constexpr (m == 14) { m1 = bflo(mi); PIN(m1); }
      if constexpr (m == 15) { m2 = __builtin_amdgcn_rcpf(m1); PIN(m2); }
      if constexpr (m == 16) { m3 = m1 * m1; PIN(m3); }
      if constexpr (m == 17) { m1 = m2 * k0c; PIN(m1); }
      if constexpr (m == 18) { m2 = m2 * m2; PIN(m2); }
      if constexpr (m == 19) { m2 = m2 * 0.044715f; PIN(m2); }
      if constexpr (m == 20) { m2 = m2 * m1; PIN(m2); }
      if constexpr (m >= 21 && m < 25) {
        const float x = m == 21 ? m0 : m == 22 ? m3 : m == 23 ? m1 : m2;
        asm volatile("v_mov_b32 %0, %1" : "=v"(mc[m - 21]) : "v"(x));
      }
      if constexpr (m >= 25) {
        const float x = m == 25 ? m0 : m == 26 ? m3 : m == 27 ? m1 : m2;
        constexpr int e = m == 25 ? 0 : m == 26 ? 3 : m == 27 ? 1 : 2;
        const auto r = __builtin_amdgcn_permlane32_swap(__float_as_uint(x), __float_as_uint(mc[m - 25]), false, false);
        float a = __uint_as_float(r[0]), b = __uint_as_float(r[1]);
        PIN(a); PIN(b);
        cst[set][0][e] = a; cst[set][1][e] = b;
      }
    } else {
      constexpr int e = id - OP_TS, i = e / 5, st = e % 5, H = (q - 1) >> 1;
      u32* d = pk[H][i];
      if constexpr (st < 2) {
        const auto r = __builtin_amdgcn_permlane32_swap(d[st], d[st + 2], false, false);
        d[st] = r[0]; d[st + 2] = r[1]; PIN(d[st]); PIN(d[st + 2]);
      } else if constexpr (st < 4) {
        constexpr int b = 2 * (st - 2);
        const auto r = __builtin_amdgcn_permlane16_swap(d[b], d[b + 1], false, false);
        d[b] = r[0]; d[b + 1] = r[1]; PIN(d[b]); PIN(d[b + 1]);
      } else {
        *(u32x4*)(hhp + (size_t)H * 4 * 49152 + i * 256) = (u32x4){d[0], d[1], d[2], d[3]};
      }
    }
  };
  WAIT_VM(KQ.issued[0] - KQ.idxH[0] - 1);
  SBAR();
  STAMP(2);
  sfor<4>([&](auto J) { rd(IC<0>{}, J); asm volatile("" ::: "memory"); });
  sfor<4>([&](auto J) { rd(IC<1>{}, J); asm volatile("" ::: "memory"); });
  sfor<4>([&](auto Q) {
    sfor<20>([&](auto S) {
      constexpr int q = Q, s = S, T = 20 * q + s, nm = s < 10 ? 8 : 6, cur = T % 3;
      if constexpr (q == 0) WAITB_VM(KQ.issued[T] - KQ.idxW[s] - 1);
      sfor<nm>([&](auto X) {
        constexpr int x = X;
        const u32x4 B = mk4(Bl[cur][x & 1 & (x < 6)], Bh[cur][x & 1 & (x < 6)]);
        if constexpr (x < 6) {
          if constexpr (s == 0) mfma_av0(acc[q & 1][x], W[s][x >> 1], B); else mfma_av(acc[q & 1][x], W[s][x >> 1], B);
        } else if constexpr (x == 6) {
          if constexpr (s == 0) mfma_vv0(sg, B, B); else mfma_vv(sg, B, B);
        } else {
          if constexpr (s == 0) mfma_vv0(so, ones, B); else mfma_vv(so, ones, B);
        }
        if constexpr (x == 1) {
          if constexpr (T <= 6 && T % 2 == 0) { WAIT_VM(KQ.issued[T] - KQ.idxH[T / 2 + 1] - 1); SBAR(); }
          if constexpr (T >= 18 && T <= 26 && T % 2 == 0) { WAIT_VM(KQ.issued[T] - KQ.idxQ1[(T - 18) / 2] - 1); SBAR(); }
          if constexpr (s == 13) {
            if constexpr (q == 1) WAIT_VM(KQ.issued[T] - KQ.idxQ2 - 1);
            if constexpr (q == 2) WAIT_VM(KQ.issued[T] - KQ.idxQ3 - 1);
            SBAR();
            sv = *(LDS_AS f32x4*)(str + (q == 0 ? 162816 : (q - 1) * 1024));
          }
          if constexpr (T < 8) ldW(IC<12 + T>{});
          else if constexpr (T < 13) ldH(1, T - 8);
          else if constexpr (T < 19) {
            constexpr int qz = (T - 13) / 3 + 2, pz = (T - 13) % 3;
            const u32 o = qz * 40960 + w * 10240 + pz * 4096;
            if constexpr (pz < 2) dma4(smb + o, rw, l16, hsrc + o); else dma2(smb + o, rw, l16, hsrc + o);
          }
          if constexpr (T == 30 || T == 40) {
            constexpr int hs = T == 40;
            bp[hs][0] += 81920; bp[hs][1] += 81920;
            asm volatile("" : "+v"(bp[hs][0]), "+v"(bp[hs][1]));
          }
          if constexpr (T == 10) {
            asm volatile("" : "+v"(csr[0][0]), "+v"(csr[0][1]), "+v"(csr[0][2]), "+v"(csr[1][0]), "+v"(csr[1][1]), "+v"(csr[1][2]));
#pragma unroll
            for (int i = 0; i < 3; ++i) { cs[i] = __builtin_bit_cast(f32x4, csr[0][i]) + __builtin_bit_cast(f32x4, csr[1][i]); PIN(cs[i]); }
          }
        }
        if constexpr (x >= 2 && x < 6 && T + 2 < 80) rd(IC<T + 2>{}, IC<x - 2>{});
        SCHED0();
        sfor<ksched<q>().n[8 * s + x]>([&](auto O) { op(Q, IC<ksched<q>().op[8 * s + x][O]>{}); SCHED0(); });
        SCHED0();
      });
    });
    if constexpr (Q < 3) STAMP(3 + Q);
  });
  asm volatile("s_nop 7" : "+v"(acc[1][0]), "+v"(acc[1][1]), "+v"(acc[1][2]), "+v"(acc[1][3]), "+v"(acc[1][4]), "+v"(acc[1][5]));
  STAMP(6);
#pragma unroll
  for (int t = 0; t < 6; ++t) {
    const int i = t >> 1, jj = t & 1;
    const f32x4 a = acc[1][t];
    const float nmu = cst[1][jj][0], K0 = cst[1][jj][1], K1 = cst[1][jj][2], ir2 = cst[1][jj][3];
    const f32x2 G = (f32x2){cs[i].x, cs[i].y} * nmu + (f32x2){a.x, a.y};
    const f32x2 U = (f32x2){cs[i].z, cs[i].w} * nmu + (f32x2){a.z, a.w};
    const f32x2 wv = ((G * G) * K1 + K0) * G;
    f32x2 e; e.x = __builtin_amdgcn_exp2f(wv.x); e.y = __builtin_amdgcn_exp2f(wv.y);
    const f32x2 dd = e * ir2 + ir2;
    f32x2 rc; rc.x = __builtin_amdgcn_rcpf(dd.x); rc.y = __builtin_amdgcn_rcpf(dd.y);
    const f32x2 hv = (G * U) * rc;
    pk[1][i][2 + jj] = pack2(hv.x, hv.y);
    if (jj == 1) {
      u32* d = pk[1][i];
      const auto r0 = __builtin_amdgcn_permlane32_swap(d[0], d[2], false, false);
      const auto r1 = __builtin_amdgcn_permlane32_swap(d[1], d[3], false, false);
      const auto r2 = __builtin_amdgcn_permlane16_swap(r0[0], r1[0], false, false);
      const auto r3 = __builtin_amdgcn_permlane16_swap(r0[1], r1[1], false, false);
      *(u32x4*)(hhp + (size_t)4 * 49152 + i * 256) = (u32x4){r2[0], r2[1], r3[0], r3[1]};
    }
  }
  STAMP(7);
  PROF_END(2);
}

constexpr int K3_RES = 61440, K3_LDS = K3_RES + 10240;
constexpr int k3_issued(int T) { const int n = 60 + 6 * T; return n > 111 ? 111 : n; }
constexpr int k3_need(int T) { return T < 12 ? 6 * T + 5 : T < 24 ? 72 + (T - 12) : 87 + (T - 24); }

__global__ __launch_bounds__(256, 1) void k3_gemm2(char* __restrict__ ws, u16* __restrict__ out) {
  extern __shared__ __attribute__((aligned(16))) char smraw[];
  LDS_AS char* sm = (LDS_AS char*)smraw;
  const u32 smb = (u32)(size_t)sm;
  const int bid = blockIdx.x, tid = threadIdx.x, lane = tid & 63;
  const int w = __builtin_amdgcn_readfirstlane(tid >> 6);
  PROF_DECL;
  const int xcd = bid & 7, jb = bid >> 3;
  const int pt = 8 * (xcd >> 1) + (jb & 7), dtile = 4 * (xcd & 1) + (jb >> 3);
  const u32x4 rw = mkrsrc(ws, WS_END);
  const u32 l16 = lane * 16;
  const u32 asrc = WS_WP2 + (dtile * 48 + 12 * w) * 5120;
  u32 bsrc[4];
#pragma unroll
  for (int s = 0; s < 4; ++s) bsrc[s] = WS_HH + ((4 * pt + ((w + 1 + s) & 3)) * 48 + 12 * w) * 1024;
  u32x4 A[12][5], B[4][12];
  auto item = [&](auto N) {
    constexpr int nn = N;
    if constexpr (nn < 72) {
      constexpr int k = nn / 6, r = nn % 6;
      if constexpr (r == 0) ld_v<1024 * (k & 3)>(B[0][k], rw, l16, bsrc[0] + (k >> 2) * 4096);
      else if constexpr (r < 5) ld_a<1024 * (r - 1)>(A[k][r - 1], rw, l16, asrc + k * 5120);
      else ld_a<0>(A[k][4], rw, l16, asrc + k * 5120 + 4096);
    } else if constexpr (nn >= 84 && nn < 87) {
      const u32 o = (w & 1) * 2048 + (nn - 84) * 1024;
      dma1(smb + K3_RES + (w >> 1) * 5120 + o, rw, l16, WS_HT + ((2 * pt + (w >> 1)) * D_ + 80 * dtile) * 64 + o);
    } else if constexpr (nn < 111) {
      constexpr int mm = nn < 84 ? nn - 60 : nn - 63, s = mm / 12, k = mm % 12;
      ld_v<1024 * (k & 3)>(B[s][k], rw, l16, bsrc[s] + (k >> 2) * 4096);
    }
  };
  sfor<60>([&](auto N) { item(N); });
  STAMP(1);
  const int n = lane & 15, g = lane >> 4, qq = n >> 2, pp = lane & 3;
  LDS_AS char* pw[3];
#pragma unroll
  for (int s = 0; s < 3; ++s) pw[s] = sm + ((((w + 1 + s) & 3) * 3 + s) * 5) * 1024 + l16;
  LDS_AS char* pr = sm + w * 15360 + l16;
  LDS_AS char* rp0 = sm + K3_RES + (w >> 1) * 5120 + (8 * g + qq) * 64 + 8 * pp;
  LDS_AS char* rp4 = sm + K3_RES + (w >> 1) * 5120 + (64 + 4 * g + qq) * 64 + 32 * ((w ^ g) & 1) + 8 * pp;
  const int P = 64 * pt + 16 * w + n;
  const int srow = 512 * (P >> 9) + 256 * ((P >> 2) & 1) + 32 * ((P >> 6) & 7) + 16 * ((P >> 1) & 1) + 2 * ((P >> 3) & 7) + (P & 1);
  u16* op = out + (size_t)srow * D_ + 80 * dtile;
  f32x4 acc[2][5], pre[5], p0[5], p1[5];
  u32x2 rs[5];
  sfor<4>([&](auto S) {
    sfor<12>([&](auto K) {
      constexpr int s = S, k = K, T = 12 * s + k;
      WAITB_VM(k3_issued(T) - k3_need(T) - 1);
      sfor<5>([&](auto I) {
        constexpr int i = I;
        if constexpr (k == 0) mfma_av0(acc[s & 1][i], A[k][i], B[s][k]); else mfma_av(acc[s & 1][i], A[k][i], B[s][k]);
        if constexpr (60 + 6 * T + i < 111) item(IC<60 + 6 * T + i>{});
        if constexpr (i == 4 && 60 + 6 * T + 5 < 111) item(IC<60 + 6 * T + 5>{});
        if constexpr (s >= 1 && k == 1) *(LDS_AS f32x4*)(pw[s - 1] + i * 1024) = acc[(s - 1) & 1][i];
        if constexpr (s >= 2 && k == 3 && i == 0) { WAIT_LGKM0(); SBAR(); }
        if constexpr (s == 2) {
          if constexpr (k == 3) p0[i] = *(LDS_AS f32x4*)(pr + i * 1024);
          if constexpr (k == 4) p1[i] = *(LDS_AS f32x4*)(pr + (5 + i) * 1024);
          if constexpr (k == 5) rs[i] = i < 4 ? tr_read(rp0 + (32 * (i >> 1) + 4 * (i & 1)) * 64 + 32 * ((w ^ i) & 1)) : tr_read(rp4);
          if constexpr (k == 6) { pre[i].x = p0[i].x + p1[i].x; PIN(pre[i].x); SCHED0(); pre[i].y = p0[i].y + p1[i].y; PIN(pre[i].y); }
          if constexpr (k == 7) { pre[i].z = p0[i].z + p1[i].z; PIN(pre[i].z); SCHED0(); pre[i].w = p0[i].w + p1[i].w; PIN(pre[i].w); }
          if constexpr (k == 8) { p1[i].x = bflo(rs[i].x); PIN(p1[i].x); SCHED0(); p1[i].y = bfhi(rs[i].x); PIN(p1[i].y); }
          if constexpr (k == 9) { p1[i].z = bflo(rs[i].y); PIN(p1[i].z); SCHED0(); p1[i].w = bfhi(rs[i].y); PIN(p1[i].w); }
          if constexpr (k == 10) { pre[i].x += p1[i].x; PIN(pre[i].x); SCHED0(); pre[i].y += p1[i].y; PIN(pre[i].y); }
          if constexpr (k == 11) { pre[i].z += p1[i].z; PIN(pre[i].z); SCHED0(); pre[i].w += p1[i].w; PIN(pre[i].w); }
        }
        if constexpr (s == 3) {
          if constexpr (k == 3) p0[i] = *(LDS_AS f32x4*)(pr + (10 + i) * 1024);
          if constexpr (k == 6) { pre[i].x += p0[i].x; PIN(pre[i].x); SCHED0(); pre[i].y += p0[i].y; PIN(pre[i].y); }
          if constexpr (k == 7) { pre[i].z += p0[i].z; PIN(pre[i].z); SCHED0(); pre[i].w += p0[i].w; PIN(pre[i].w); }
        }
        SCHED0();
      });
    });
    STAMP(2 + S);
  });
  asm volatile("s_nop 7" : "+v"(acc[1][0]), "+v"(acc[1][1]), "+v"(acc[1][2]), "+v"(acc[1][3]), "+v"(acc[1][4]));
  u32x2 o[5];
#pragma unroll
  for (int i = 0; i < 5; ++i) {
    const f32x4 t = acc[1][i] + pre[i];
    o[i] = (u32x2){pack2(t.x, t.y), pack2(t.z, t.w)};
  }
  *(u32x4*)(op + 8 * g) = mk4(o[0], o[1]);
  *(u32x4*)(op + 32 + 8 * g) = mk4(o[2], o[3]);
  *(u32x2*)(op + 64 + 4 * g) = o[4];
  STAMP(6);
  PROF_END(3);
}

extern "C" void kernel_launch(void* const* d_in, const int* in_sizes, int n_in, void* d_out, int out_size, void* d_ws,
                              size_t ws_size, hipStream_t stream) {
  const u16* q = (const u16*)d_in[0];
  const u16* k = (const u16*)d_in[1];
  const u16* v = (const u16*)d_in[2];
  const u16* shk = (const u16*)d_in[3];
  const u16* ssk = (const u16*)d_in[4];
  const u16* ssD = (const u16*)d_in[5];
  const u16* wg = (const u16*)d_in[7];
  const u16* wu = (const u16*)d_in[8];
  const u16* wd = (const u16*)d_in[9];
  char* ws = (char*)d_ws;
  (void)hipFuncSetAttribute((const void*)k1_conv, hipFuncAttributeMaxDynamicSharedMemorySize, K1_LDS);
  (void)hipFuncSetAttribute((const void*)k2_gemm1, hipFuncAttributeMaxDynamicSharedMemorySize, K2_LDS);
  (void)hipFuncSetAttribute((const void*)k3_gemm2, hipFuncAttributeMaxDynamicSharedMemorySize, K3_LDS);
  k0_prep<<<448, 256, 0, stream>>>(q, k, v, shk, ws);
  k1_conv<<<256, 256, K1_LDS, stream>>>(ssk, ssD, wg, wu, wd, ws);
  k2_gemm1<<<256, 256, K2_LDS, stream>>>(ws);
  k3_gemm2<<<256, 256, K3_LDS, stream>>>(ws, (u16*)d_out);
}
```

```cpp
#include <hip/hip_runtime.h>
#include <utility>

typedef unsigned short u16;
typedef unsigned int u32;
typedef short s16x4 __attribute__((ext_vector_type(4)));
typedef __bf16 bf16x2 __attribute__((ext_vector_type(2)));
typedef float f32x2 __attribute__((ext_vector_type(2)));
typedef float f32x4 __attribute__((ext_vector_type(4)));
typedef u32 u32x2 __attribute__((ext_vector_type(2)));
typedef u32 u32x4 __attribute__((ext_vector_type(4)));
typedef u32 u32x8 __attribute__((ext_vector_type(8)));
#define LDS_AS __attribute__((address_space(3)))

constexpr int S_ = 2048, D_ = 640, F_ = 1536;

#define FS1 8
#define FS2 4
#define FS3 128
#define FS4 32
constexpr u32 CT_E = 0, CT_FS = 4096, CT_FD = CT_FS + 64 * 128, CT_F3 = CT_FD + 384 * 128, CT_H = CT_F3 + 320 * 128;
#define D3 (160 * 128)
#define D4 (1024 * 128)
constexpr u32 OOB = 0x80000000u;
#define WSL 1
constexpr u32 WS_ST1 = CT_H + 96 * 128;
constexpr u32 WS_KV = WS_ST1 + 16384;
constexpr u32 WS_XP = WS_KV + D_ * S_;
constexpr u32 WS_HT = WS_XP + D_ * S_ * 2;
constexpr u32 WS_WP2 = WS_HT + D_ * S_ * 2;
constexpr u32 CT_F4 = WS_WP2 + D_ * F_ * 2;
constexpr u32 WS_HH = CT_F4 + 4096 * 128;
constexpr u32 WS_END = WS_HH + S_ * F_ * 2;

__device__ __forceinline__ float bflo(u32 x) { return __uint_as_float(x << 16); }
__device__ __forceinline__ float bfhi(u32 x) { return __uint_as_float(x & 0xffff0000u); }
__device__ __forceinline__ u32 pack2(float lo, float hi) {
  bf16x2 v; v.x = (__bf16)lo; v.y = (__bf16)hi;
  return __builtin_bit_cast(u32, v);
}
__device__ __forceinline__ float rbf(float f) { __bf16 b = (__bf16)f; return (float)b; }
__device__ __forceinline__ u32x2 tr_read(LDS_AS char* p) {
  s16x4 v = __builtin_amdgcn_ds_read_tr16_b64_v4i16((LDS_AS s16x4*)p);
  return __builtin_bit_cast(u32x2, v);
}
__device__ __forceinline__ u32x4 mk4(u32x2 a, u32x2 b) { return (u32x4){a.x, a.y, b.x, b.y}; }
__device__ __forceinline__ u32x4 mkrsrc(const void* p, u32 nbytes) {
  const unsigned long long a = (unsigned long long)p;
  return (u32x4){(u32)a, (u32)(a >> 32) & 0xffffu, nbytes, 0x00020000u};
}
#define OUT_POL ""
#define WAIT_VM(n) asm volatile("s_waitcnt vmcnt(%0)" ::"n"(n) : "memory")
#define WAITB_VM(n) __builtin_amdgcn_s_waitcnt(((n) & 15) | 0xf70 | (((n) >> 4) << 14))
#define WAITB_LG(n) __builtin_amdgcn_s_waitcnt(0xc07f | ((n) << 8))
#define WAIT_LGKM0() asm volatile("s_waitcnt lgkmcnt(0)\n\th3chk" ::: "memory")
#define SBAR() asm volatile("s_barrier\n\th3chk" ::: "memory")
#define SCHED0() __builtin_amdgcn_sched_barrier(0)
#define COLD(x) __builtin_expect(x, 0)
#define AI __attribute__((always_inline))
#define PIN(x) asm volatile("" : "+v"(x))
#define PINS(x) asm volatile("" : "+s"(x))
template <int N> using IC = std::integral_constant<int, N>;
template <class Fn, int... I> __device__ __forceinline__ void sfor_impl(Fn&& f, std::integer_sequence<int, I...>) {
  (f(std::integral_constant<int, I>{}), ...);
}
template <int N, class Fn> __device__ __forceinline__ void sfor(Fn&& f) { sfor_impl(f, std::make_integer_sequence<int, N>{}); }

#define PGS "\n\th3chk"
#define PGCHK() asm volatile("h3chk")
#define PG_DEF \
  ".macro h3pad n, m\n.Lh3_p\\n:\n\ts_cbranch_execz .Lh3_p\\m\n.endm\n" \
  ".macro h3chk\n.altmacro\n.set h3pg, (. - h3_fused) >> 12\n.if h3pg != h3last\n.set h3last, h3pg\n" \
  ".if h3pg == 1\nh3pad 1, 0\n.elseif h3pg < 8\nh3pad %h3pg, %(h3pg + 4)\n.endif\n.endif\n.endm\n.set h3last, 0\n"
#define PG_GO(a, b, c, d) \
  "s_getpc_b64 vcc\n.Lh3_v%=:\n\ts_add_u32 vcc_lo, vcc_lo, .Lh3_r%=-.Lh3_v%=\n\ts_addc_u32 vcc_hi, vcc_hi, 0\n\ts_mov_b64 exec, 0\n\t" \
  "s_cmp_eq_u32 %0, 0\n\ts_cbranch_scc1 .Lh3_p" #a "\n\ts_cmp_eq_u32 %0, 1\n\ts_cbranch_scc1 .Lh3_p" #b "\n\t" \
  "s_cmp_eq_u32 %0, 2\n\ts_cbranch_scc1 .Lh3_p" #c "\n\ts_branch .Lh3_p" #d "\n"
#define PG_BACK ".Lh3_r%=:\n\ts_mov_b64 exec, -1"
#define DMA_P(o, c) "\n\tbuffer_load_dwordx4 %1, %2, %3 offen offset:" #o c " lds" PGS
#define DMA_ASM(body) asm volatile("h3chk\n\ts_mov_b32 m0, %0\n\ts_nop 0" body ::"s"(lds), "v"(voff), "s"(rs), "s"(soff) : "memory")
template <int N, bool C> __device__ __forceinline__ void dma(u32 lds, u32x4 rs, u32 voff, u32 soff) {
  if constexpr (C) {
    if constexpr (N == 1) DMA_ASM(DMA_P(0, " sc1"));
    if constexpr (N == 2) DMA_ASM(DMA_P(0, " sc1") DMA_P(1024, " sc1"));
    if constexpr (N == 4) DMA_ASM(DMA_P(0, " sc1") DMA_P(1024, " sc1") DMA_P(2048, " sc1") DMA_P(3072, " sc1"));
  } else {
    if constexpr (N == 1) DMA_ASM(DMA_P(0, ""));
    if constexpr (N == 3) DMA_ASM(DMA_P(0, "") DMA_P(1024, "") DMA_P(2048, ""));
    if constexpr (N == 4) DMA_ASM(DMA_P(0, "") DMA_P(1024, "") DMA_P(2048, "") DMA_P(3072, ""));
  }
}
template <int AR, int IMM> __device__ __forceinline__ void ldc_a(u32x4 rs, u32 voff, u32 soff) {
  asm volatile("buffer_load_dwordx4 a[%c0:%c1], %2, %3, %4 offen offset:%5 sc1" ::"n"(AR), "n"(AR + 3), "v"(voff), "s"(rs), "s"(soff), "n"(IMM) : "memory");
}
template <int IMM> __device__ __forceinline__ void ldc_v(u32x4& d, u32x4 rs, u32 voff, u32 soff) {
  asm volatile("buffer_load_dwordx4 %0, %1, %2, %3 offen offset:%4 sc1" : "=v"(d) : "v"(voff), "s"(rs), "s"(soff), "n"(IMM) : "memory");
}
template <int IMM> __device__ __forceinline__ void ld_v(u32x4& d, u32x4 rs, u32 voff, u32 soff) {
  asm volatile("buffer_load_dwordx4 %0, %1, %2, %3 offen offset:%4" : "=v"(d) : "v"(voff), "s"(rs), "s"(soff), "n"(IMM) : "memory");
}
__device__ __forceinline__ void ld_v2(u32x2& d, u32x4 rs, u32 voff, u32 soff) {
  asm volatile("buffer_load_dwordx2 %0, %1, %2, %3 offen" : "=v"(d) : "v"(voff), "s"(rs), "s"(soff) : "memory");
}
template <int IMM> __device__ __forceinline__ void stc4(u32x4 d, u32x4 rs, u32 voff, u32 soff) {
  asm volatile("buffer_store_dwordx4 %0, %1, %2, %3 offen offset:%4 sc1\n\ts_nop 1" PGS ::"v"(d), "v"(voff), "s"(rs), "s"(soff), "n"(IMM) : "memory");
}
template <int IMM> __device__ __forceinline__ void stc2(u32x2 d, u32x4 rs, u32 voff, u32 soff) {
  asm volatile("buffer_store_dwordx2 %0, %1, %2, %3 offen offset:%4 sc1" ::"v"(d), "v"(voff), "s"(rs), "s"(soff), "n"(IMM) : "memory");
}
__device__ __forceinline__ void stc1(u32 d, u32x4 rs, u32 voff, u32 soff) {
  asm volatile("buffer_store_dword %0, %1, %2, %3 offen sc1" PGS ::"v"(d), "v"(voff), "s"(rs), "s"(soff) : "memory");
}
__device__ __forceinline__ void ldf(u32& d, u32x4 rs, u32 voff, u32 soff) {
  asm volatile("buffer_load_dword %0, %1, %2, %3 offen sc1" : "=v"(d) : "v"(voff), "s"(rs), "s"(soff) : "memory");
}
__device__ __forceinline__ void ldf2(u32x2& d, u32x4 rs, u32 voff) {
  asm volatile("buffer_load_dwordx2 %0, %1, %2, 0 offen sc1" : "=v"(d) : "v"(voff), "s"(rs) : "memory");
}
__device__ __forceinline__ void touch(u32x4 rs, u32 voff, u32 soff) {
  asm volatile("buffer_load_dword a255, %0, %1, %2 offen" ::"v"(voff), "s"(rs), "s"(soff) : "memory");
}
template <int SL = 1> __device__ __forceinline__ void spin(u32x4 rs, u32 vo, u32 so, u32 tag) {
  so = __builtin_amdgcn_readfirstlane(so);
#pragma nounroll
  for (int it = 0; it < 200000; ++it) {
    u32 a;
    asm volatile("buffer_load_dword %0, %1, %2, %3 offen sc1\n\ts_waitcnt vmcnt(0)" PGS : "=&v"(a) : "v"(vo), "s"(rs), "s"(so) : "memory");
    vo = a == tag ? OOB : vo;
    if (__builtin_amdgcn_ballot_w64(vo != OOB) == 0ull) break;
    __builtin_amdgcn_s_sleep(SL);
  }
}
__device__ __forceinline__ void spin2(u32x4 rs, u32 vo, u32 tag) {
#pragma nounroll
  for (int it = 0; it < 200000; ++it) {
    u32x2 a;
    asm volatile("buffer_load_dwordx2 %0, %1, %2, 0 offen sc1\n\ts_waitcnt vmcnt(0)" PGS : "=&v"(a) : "v"(vo), "s"(rs) : "memory");
    vo = a.x == tag && a.y == tag ? OOB : vo;
    if (__builtin_amdgcn_ballot_w64(vo != OOB) == 0ull) break;
    __builtin_amdgcn_s_sleep(1);
  }
}
__device__ __forceinline__ float dot4(u32 a, u32 b, u32 c, u32 d) {
  float r;
  asm volatile("v_dot2_f32_bf16 %0, %1, %2, 0\n\tv_dot2_f32_bf16 %0, %3, %4, %0" : "=&v"(r) : "v"(a), "s"(b), "v"(c), "s"(d));
  return r;
}
__device__ __forceinline__ bool allset(u32& a, u32 tag) {
  PIN(a);
  return __builtin_amdgcn_ballot_w64(a != tag) == 0ull;
}
__device__ __forceinline__ bool allset2(u32x2& a, u32 tag) {
  PIN(a);
  return (__builtin_amdgcn_ballot_w64(a.x != tag) | __builtin_amdgcn_ballot_w64(a.y != tag)) == 0ull;
}
__device__ __forceinline__ float dot2(u32 a, u32 b, float c) {
  return __builtin_amdgcn_fdot2_f32_bf16(__builtin_bit_cast(bf16x2, a), __builtin_bit_cast(bf16x2, b), c, false);
}
template <bool Z> __device__ __forceinline__ void mfma_vv(f32x4& c, const u32x4& a, const u32x4& b) {
  if constexpr (Z) asm volatile("v_mfma_f32_16x16x32_bf16 %0, %1, %2, 0" PGS : "=&v"(c) : "v"(a), "v"(b) : "memory");
  else asm volatile("v_mfma_f32_16x16x32_bf16 %0, %1, %2, %0" PGS : "+v"(c) : "v"(a), "v"(b) : "memory");
}
template <bool Z, int AR> __device__ __forceinline__ void mfma_av(f32x4& c, const u32x4& b) {
  if constexpr (Z) asm volatile("v_mfma_f32_16x16x32_bf16 %0, a[%c2:%c3], %1, 0" PGS : "=&v"(c) : "v"(b), "n"(AR), "n"(AR + 3) : "memory");
  else asm volatile("v_mfma_f32_16x16x32_bf16 %0, a[%c2:%c3], %1, %0" PGS : "+v"(c) : "v"(b), "n"(AR), "n"(AR + 3) : "memory");
}
template <bool Z, int BR> __device__ __forceinline__ void mfma_va(f32x4& c, const u32x4& a) {
  if constexpr (Z) asm volatile("v_mfma_f32_16x16x32_bf16 %0, %1, a[%c2:%c3], 0" PGS : "=&v"(c) : "v"(a), "n"(BR), "n"(BR + 3) : "memory");
  else asm volatile("v_mfma_f32_16x16x32_bf16 %0, %1, a[%c2:%c3], %0" PGS : "+v"(c) : "v"(a), "n"(BR), "n"(BR + 3) : "memory");
}
template <bool Z, int AR, int BR> __device__ __forceinline__ void mfma_aa(f32x4& c) {
  if constexpr (Z) asm volatile("v_mfma_f32_16x16x32_bf16 %0, a[%c1:%c2], a[%c3:%c4], 0" PGS : "=&v"(c) : "n"(AR), "n"(AR + 3), "n"(BR), "n"(BR + 3) : "memory");
  else asm volatile("v_mfma_f32_16x16x32_bf16 %0, a[%c1:%c2], a[%c3:%c4], %0" PGS : "+v"(c) : "n"(AR), "n"(AR + 3), "n"(BR), "n"(BR + 3) : "memory");
}

#define PROF_DECL
#define STAMP(i)
#define STAMPC(i)
#define STAMPT(i)
#define STAMPG(g)
#define PROF_END()

#define STAMPA(i) STAMP(i)
#define STAMPB(i)

#define R_KV 0
#define S_KV 2
#define R_N0 3
#define S_LAG 2
#define CT_ON 2
#define PG_WA 0
#define PG_GB 110
#define CT_SEL (bid >= 224)

constexpr int L_ST = 0;
constexpr int L_F = 16384, L_FW = 8960, L_FB = 4480;
constexpr int L_G = L_F + 35840, L_GW = 8576, L_GB = 4288;
constexpr int L_S = 40960, L_SP = 24576;
constexpr int L_KK = L_S, L_KV = L_KK + 45 * 1088;
constexpr int L_PQ = 0, L_WD = 0;
constexpr int L_PART = 46080, L_PW = 5376;
constexpr int LDS_BYTES = 163840;
static_assert(L_G + 34304 <= L_S + 2 * L_SP && L_WD + 20480 <= L_S && L_S + 5 * L_SP == LDS_BYTES, "lds");

struct PairTab { int kk[36], m[36], n; };
constexpr PairTab make_pairs(int hi) {
  PairTab t{}; int n = 0;
  for (int d = 0; d < 8; ++d) {
    for (int e = 0; e < d; ++e) if (d < 4 && d + e <= hi) { t.kk[n] = e; t.m[n] = d; ++n; }
    for (int e = 0; e <= d; ++e) if (e < 4 && d + e <= hi) { t.kk[n] = d; t.m[n] = e; ++n; }
  }
  t.n = n;
  return t;
}
constexpr PairTab PA = make_pairs(3), PB = make_pairs(7);
static_assert(PA.n == 10 && PB.n == 26, "pairs");
constexpr bool first_in_phase(const PairTab& t, int j) {
  for (int i = 0; i < j; ++i) if (t.kk[i] + t.m[i] == t.kk[j] + t.m[j]) return false;
  return true;
}

#define G_W0 38
#define G_FL1 116
#define G_FL2 130
#define G_CHK 138
#define G_WS 5
#define G_B0 110
#define G_B 248
#define H_SLP 8
#define NC_SLP 4
#define NC_SLP2 4
constexpr int G_A = 182;
#define G_KW 168
#define G_XS 2
constexpr int gbar[8] = {G_B0, G_CHK, G_A, 208, 228, 248, 268, 288};
constexpr int bar_at(int gi) { for (int j = 0; j < 7; ++j) if (gbar[j] == gi) return j; return -1; }
constexpr int xs_at(int gi) { return gi > G_CHK && (gi - G_CHK - 1) % G_XS == 0 && (gi - G_CHK - 1) / G_XS < 10 ? (gi - G_CHK - 1) / G_XS : -1; }
constexpr int wl_gap(int i) {
  return i < 18 ? G_W0 + G_WS * i : i < 24 ? G_CHK + 1 + 10 * G_XS + 3 * (i % 6) : gbar[i / 6 - 2] + 1 + (i < 30 ? 4 : 3) * (i % 6);
}
constexpr int wl_at(int gi) { for (int i = 0; i < 54; ++i) if (wl_gap(i) == gi) return i; return -1; }
constexpr int wr_gap(int i) { return gbar[i / 12] + i % 12 + (i < 24 ? 7 : i < 36 ? 9 : 3); }
constexpr int wr_at(int gi) { for (int i = 0; i < 84; ++i) if (wr_gap(i) == gi) return i; return -1; }
static_assert(wl_gap(17) < G_CHK && wl_gap(5) < G_B0 && wl_gap(23) < G_A && bar_at(G_B) >= 5 && wr_gap(11) < G_CHK, "weight loads");
struct CQ { int before[289]; int kv, fl1, fl2, xv3, hs, wl[10]; };
constexpr CQ make_cq() {
  CQ t{}; int n = 0;
  for (int gi = 0; gi < 288; ++gi) {
    t.before[gi] = n;
    if (gi == G_B) ++n;
    if (gi == G_FL1) t.fl1 = n++;
    if (gi == G_FL2) t.fl2 = n++;
    if (xs_at(gi) == 1) t.kv = n;
    if (xs_at(gi) >= 0) t.xv3 = n++;
    if (wl_at(gi) >= 0) t.wl[wl_at(gi) / 6] = n++;
    if (gi == 208 + 6 || gi == 208 + 12) t.hs = n++;
  }
  t.before[288] = n;
  return t;
}
constexpr CQ C_ = make_cq();
constexpr int cw(int gi, int seq) { return C_.before[gi] - seq - 1; }
constexpr int imax(int a, int b) { return a > b ? a : b; }

constexpr int NWV = 32;
#define T_FB 20
#define Q3_END 15
#define TL_FS 3
#define P3_FS 11
#define P3_L2 10
#define P3_C2 16
#define P3_L3 24
#define P3_C3 28
#define SC_LA 2
#define SC_M 4
constexpr int NWA = 21;
constexpr int nm_of(int T) { return T % 20 < 10 ? 8 : 6; }
constexpr int hd_at(int T, int x) { return x != 1 ? -1 : T < 10 ? 10 + T : (T >= T_FB && T < T_FB + 20) ? 20 + T - T_FB : -1; }
constexpr int wv_at(int T, int x) { return x != (T % 20 < 10 ? 5 : 4) ? -1 : T < 20 ? T : (T >= 40 && T < 20 + NWV) ? T - 20 : -1; }
constexpr int wa_at(int T, int x) {
  if (T >= 61 && T < 71) return x == 1 ? 2 * (T - 61) : x == 5 ? 2 * (T - 61) + 1 : -1;
  return T == 71 && x == 3 ? 20 : -1;
}
constexpr int hb_at(int T, int x) { return T >= 72 && T < 80 && (6 * (T - 72) + x) % 4 == 1 ? (6 * (T - 72) + x) / 4 : -1; }
constexpr int fs_at(int T, int x) { return x != 6 ? -1 : T == 0 ? 4 : T == 47 ? 0 : T == 67 ? 1 : -1; }
constexpr int fl_at(int T, int x) { return x == 1 && T == T_FB - 4 ? 4 : x != 3 ? -1 : T == 66 ? 0 : T == 78 ? 1 : -1; }
constexpr int ds_at(int T, int x) { return T < 78 ? x >= 2 && x < 6 : T == 78 ? x == 2 || x == 4 : x == 1 || x == 2 || x == 4; }
constexpr int hb3_at(int T, int i) {
  if (T < 12) return i == 0 ? 12 + T : -1;
  if (i != 0 && i != 3) return -1;
  if (T >= P3_C2 && T < P3_C2 + 6) return 24 + 2 * (T - P3_C2) + (i == 3);
  if (T >= P3_C3 && T < P3_C3 + 6) return 36 + 2 * (T - P3_C3) + (i == 3);
  return -1;
}
static_assert(T_FB >= 20 && T_FB < 24, "ring");
constexpr int gap_cap(int T, int x) {
  if (x == 1 && (T == 18 || T == 38 || T % 20 == 13 || T == T_FB || T == 71)) return 0;
  if (x == 0 && (T == 30 || T == 40)) return 0;
  const int c = 2 - ds_at(T, x) - (hd_at(T, x) >= 0) - (wv_at(T, x) >= 0) - (wa_at(T, x) >= 0) - (hb_at(T, x) >= 0) - (fl_at(T, x) >= 0)
                - 2 * (fs_at(T, x) >= 0);
  return c < 0 ? 0 : c;
}
constexpr int OP_SW = 24, OP_CH = 36, OP_PK = 132, OP_ST = 138, OP_EXT = 140, OP_MATH = 144, OP_RA = 167, OP_END = 173;
constexpr int op_cost(int id) {
  if (id >= OP_SW && id < OP_CH) return 2;
  if (id >= OP_CH && id < OP_PK) return ((id - OP_CH) % 8 == 3 || (id - OP_CH) % 8 == 5) ? 2 : 1;
  if (id == OP_ST || id == OP_ST + 1) return 2;
  if (id == OP_EXT + 3) return 3;
  if (id >= OP_MATH && id < OP_RA) { const int m = id - OP_MATH; return (m == 13 || m == 14 || m == 17) ? 2 : 1; }
  return 1;
}
constexpr bool op_mem(int id) { return id == OP_ST || id == OP_ST + 1 || id == OP_EXT + 3; }
struct Deps { int n, d[4], dist; };
constexpr Deps op_deps(int id) {
  if (id < OP_SW) return {0, {}, 2};
  if (id < OP_CH) return {2, {2 * (id - OP_SW), 2 * (id - OP_SW) + 1}, 3};
  if (id < OP_PK) {
    const int v = (id - OP_CH) / 8, c = (id - OP_CH) % 8;
    if (c == 0) return v < 4 ? Deps{1, {OP_SW + v}, 2} : Deps{2, {OP_SW + v, id - 32 + 7}, 2};
    if (c == 6) return {1, {OP_SW + v}, 2};
    if (c == 7) return {2, {id - 2, id - 1}, 2};
    return {1, {id - 1}, 2};
  }
  if (id < OP_ST) return {2, {OP_CH + 16 * (id - OP_PK) + 7, OP_CH + 16 * (id - OP_PK) + 15}, 2};
  if (id == OP_ST) return {4, {OP_PK, OP_PK + 1, OP_PK + 2, OP_PK + 3}, 1};
  if (id == OP_ST + 1) return {2, {OP_PK + 4, OP_PK + 5}, 1};
  if (id == OP_EXT || id == OP_MATH || id == OP_RA) return {0, {}, 2};
  const int m = id - OP_MATH;
  const bool loose = id == OP_EXT + 1 || id == OP_EXT + 3 || m == 1 || m == 2 || m == 3 || m == 8 || m == 9 || m == 14 || m == 15 || m == 18 || m == 19 || m == 20;
  return {1, {id - 1}, loose ? 1 : 2};
}
constexpr int op_first(int id) { return id < OP_EXT ? 0 : id < OP_MATH ? 8 * 10 + 3 : id < OP_RA ? 8 * 15 : 8 * 14; }
struct K2Sched { int n[160]; int op[160][6]; bool ok; };
constexpr K2Sched make_sched(int q) {
  K2Sched t{};
  int ord[OP_END] = {}, no = 0, pos[OP_END] = {}, cnt = 0;
  if (q > 0) for (int id = OP_PK; id < OP_EXT; ++id) ord[no++] = id;
  for (int id = OP_EXT; id < (q == 3 ? OP_END : OP_RA); ++id) ord[no++] = id;
  if (q > 0)
    for (int v = -SC_LA; v < 12; v += 2) {
      for (int u = v + SC_LA; u < v + SC_LA + 2 && u < 12; ++u) { ord[no++] = 2 * u; ord[no++] = 2 * u + 1; ord[no++] = OP_SW + u; }
      for (int c = 0; c < 16 && v >= 0; ++c) ord[no++] = OP_CH + 8 * (v + (c & 1)) + c / 2;
    }
  for (int i = 0; i < OP_END; ++i) pos[i] = -1;
  const int vend = q == 3 ? 8 * Q3_END : 160;
  int cap[160] = {}, cost[OP_END] = {}, first[OP_END] = {}, need = 0, have = 0, needv = 0, havev = 0, lo = 0;
  Deps dep[OP_END] = {};
  for (int g = 0; g < 160; ++g) if (g % 8 < nm_of(g / 8)) { cap[g] = gap_cap(20 * q + g / 8, g % 8); have += cap[g]; if (g < vend) havev += cap[g]; }
  for (int i = 0; i < no; ++i) {
    const int id = ord[i];
    cost[id] = op_cost(id); first[id] = op_first(id); dep[id] = op_deps(id);
    need += cost[id];
    if (first[id] < vend - 8) needv += cost[id];
  }
  for (int gap = 0; gap < 160; ++gap) {
    int fr = cap[gap];
    if (fr == 0) continue;
    if (need + SC_M > have || needv + SC_M > havev) fr += 1;
    have -= cap[gap];
    if (gap < vend) havev -= cap[gap];
    for (int tries = 0; tries < 6 && fr > 0; ++tries) {
      int best = -1;
      for (int relax = 0; relax < 2 && best < 0; ++relax)
        for (int i = 0; i < no && i < lo + 60 && best < 0; ++i) {
          if (i == 41 && lo > 41) i = lo;
          const int id = ord[i];
          if (pos[id] >= 0 || gap < first[id]) continue;
          if (op_mem(id) ? t.n[gap] > 0 : cost[id] > fr) continue;
          bool rdy = true;
          for (int k = 0; k < dep[id].n; ++k) rdy = rdy && pos[dep[id].d[k]] >= 0 && cnt - pos[dep[id].d[k]] >= ((relax && t.n[gap] == 0) ? 1 : dep[id].dist);
          if (rdy) best = id;
        }
      if (best < 0) break;
      pos[best] = cnt++;
      t.op[gap][t.n[gap]++] = best;
      fr = op_mem(best) ? 0 : fr - cost[best];
      need -= cost[best];
      if (first[best] < vend - 8) needv -= cost[best];
      if (lo < 41) lo = 41;
      while (lo < no - 1 && pos[ord[lo]] >= 0) ++lo;
    }
  }
  t.ok = true;
  for (int i = 0; i < no; ++i) t.ok = t.ok && pos[ord[i]] >= 0;
  return t;
}
constexpr K2Sched KS0 = make_sched(0), KS1 = make_sched(1), KS2 = make_sched(2), KS3 = make_sched(3), KS[4] = {KS0, KS1, KS2, KS3};
static_assert(KS[0].ok && KS[1].ok && KS[2].ok && KS[3].ok, "schedule");

struct QM { int before[900]; int hd[4], st[4], fl[5], hb[48], wa[28]; };
constexpr QM make_qm() {
  QM t{}; int n = 0;
  t.hd[0] = 9; n = 11;
  for (int T = 0; T < 80; ++T)
    for (int x = 0; x < nm_of(T); ++x) {
      t.before[8 * T + x] = n;
      if (fs_at(T, x) >= 0) ++n;
      if (fl_at(T, x) >= 0) t.fl[fl_at(T, x)] = n++;
      if (hd_at(T, x) >= 0) t.hd[hd_at(T, x) / 10] = n++;
      if (wv_at(T, x) >= 0) ++n;
      if (wa_at(T, x) >= 0) t.wa[wa_at(T, x)] = n++;
      if (hb_at(T, x) >= 0) t.hb[hb_at(T, x)] = n++;
      for (int o = 0; o < KS[T / 20].n[8 * (T % 20) + x]; ++o) {
        const int id = KS[T / 20].op[8 * (T % 20) + x][o];
        if (id == OP_ST || id == OP_ST + 1) t.st[T / 20 - 1] = n++;
      }
    }
  t.wa[NWA] = n++;
  for (int u = 0; u < 6; ++u) {
    if (u == 3 || u == 5) t.st[3] = n++;
    t.before[640 + u] = n;
    if (u == TL_FS) ++n;
    t.wa[NWA + 1 + u] = n++;
  }
  t.before[646] = n;
  for (int T = 0; T < 48; ++T)
    for (int i = 0; i < 5; ++i) {
      t.before[648 + 5 * T + i] = n;
      if (T == P3_FS && i == 1) ++n;
      if (T == P3_L2 && i == 1) t.fl[2] = n++;
      if (T == P3_L3 && i == 1) t.fl[3] = n++;
      if (hb3_at(T, i) >= 0) t.hb[hb3_at(T, i)] = n++;
      if (T > 12 && T % 12 == 11 && i == 2) ++n;
    }
  return t;
}
constexpr QM Q_ = make_qm();
constexpr int wq(int slot, int seq) { const int d = Q_.before[slot] - seq - 1; return d > 63 ? 63 : d; }
constexpr int need3(int T) { return T < 12 && 5 * T + 4 >= NWV ? imax(Q_.hb[T], Q_.wa[5 * T + 4 - NWV]) : Q_.hb[T]; }

constexpr int O3_CV = 12, O3_PA = 32, O3_IN = 52, O3_AD = 112, O3_END = 196;
constexpr int rd3_at(int G) { return G >= 60 && G % 60 >= 30 && G % 60 < 38 ? G % 60 - 30 : -1; }
constexpr int cap3(int G) {
  const int T = G / 5, i = G % 5, k = T % 12;
  if (T >= 12 && ((k < 5 && i == 2) || (k == 5 && i == 4) || (k == 11 && i == 2))) return 0;
  if ((i == 1 && T == P3_FS) || (i == 0 && (T == P3_C2 || T == P3_C3))) return 0;
  return 2 - (hb3_at(T, i) >= 0) - (rd3_at(G) >= 0) - (i == 1 && (T == P3_L2 || T == P3_L3));
}
constexpr int first3(int id) {
  if (id < O3_CV) return id;
  if (id < O3_PA) return 8;
  if (id < O3_IN) return 58 + (id - O3_PA) / 4;
  if (id < O3_AD) { const int s = (id - O3_IN) / 20, t = (id - O3_IN) % 20 / 4; return s == 0 ? 8 : 60 * s + 5 * t + 3; }
  const int s = (id - O3_AD) / 28, a = (id - O3_AD) % 28;
  return 60 * (s + 1) + (a < 14 ? 39 : 44);
}
constexpr int last3(int id) {
  if (id < O3_PA) return 56;
  if (id < O3_IN) return 61 + 5 * ((id - O3_PA) / 4);
  if (id < O3_AD) return 60 * ((id - O3_IN) / 20) + 57;
  return 60 * ((id - O3_AD) / 28 + 1) + 56;
}
struct K3Sched { int n[240]; int op[240][2]; bool ok; };
constexpr K3Sched make_k3() {
  K3Sched t{}; t.ok = true;
  int prev = 0;
  for (int id = 0; id < O3_END; ++id) {
    int G = first3(id);
    if (id > O3_AD && prev > G) G = prev;
    while (t.n[G] >= cap3(G)) ++G;
    t.op[G][t.n[G]++] = id;
    prev = G;
    t.ok = t.ok && G <= last3(id);
  }
  return t;
}
constexpr K3Sched K3 = make_k3();
static_assert(K3.ok, "schedule");

template <int IMM> __device__ __forceinline__ void sadd(u32& d, u32 b) { asm volatile("s_add_u32 %0, %1, %2" : "=s"(d) : "s"(b), "n"(IMM) : "scc"); }
template <int IMM> __device__ __forceinline__ void smov(u32& d) { asm volatile("s_mov_b32 %0, %1" : "=s"(d) : "n"(IMM)); }
template <int IMM> __device__ __forceinline__ void sm0(u32 b) { asm volatile("s_add_u32 m0, %0, %1" ::"s"(b), "n"(IMM) : "scc", "memory"); }
template <int IMM> __device__ __forceinline__ void stc4n(u32x4 d, u32x4 rs, u32 voff, u32 soff) {
  asm volatile("buffer_store_dwordx4 %0, %1, %2, %3 offen offset:%4 sc1" ::"v"(d), "v"(voff), "s"(rs), "s"(soff), "n"(IMM) : "memory");
}
template <int OFF, bool SETM0> __device__ __forceinline__ void dmap(u32 lds, u32x4 rs, u32 voff, u32 soff) {
  if constexpr (SETM0) asm volatile("s_mov_b32 m0, %0\n\ts_nop 0\n\tbuffer_load_dwordx4 %1, %2, %3 offen offset:%4 sc1 lds" ::"s"(lds), "v"(voff), "s"(rs), "s"(soff), "n"(OFF) : "memory");
  else asm volatile("buffer_load_dwordx4 %0, %1, %2 offen offset:%3 sc1 lds" ::"v"(voff), "s"(rs), "s"(soff), "n"(OFF) : "memory");
}
template <int CTRL> __device__ __forceinline__ float dpp_add(float x) {
  return x + __uint_as_float(__builtin_amdgcn_update_dpp(0u, __float_as_uint(x), CTRL, 0xf, 0xf, true));
}
__device__ __forceinline__ void st_out(u32x4 d, u32x4 rs, u32 voff, u32 soff) {
  asm volatile("buffer_store_dwordx4 %0, %1, %2, %3 offen" OUT_POL ::"v"(d), "v"(voff), "s"(rs), "s"(soff) : "memory");
}
template <int OFF, bool SETM0> __device__ __forceinline__ void dmaq(u32 lds, u32x4 rs, u32 voff, u32 soff) {
  if constexpr (SETM0) asm volatile("s_mov_b32 m0, %0\n\ts_nop 0\n\tbuffer_load_dwordx4 %1, %2, %3 offen offset:%4 lds" ::"s"(lds), "v"(voff), "s"(rs), "s"(soff), "n"(OFF) : "memory");
  else asm volatile("buffer_load_dwordx4 %0, %1, %2 offen offset:%3 lds" ::"v"(voff), "s"(rs), "s"(soff), "n"(OFF) : "memory");
}
template <int AR, int OFF> __device__ __forceinline__ void tr_a1(u32 p) {
  asm volatile("ds_read_b64_tr_b16 a[%c1:%c2], %0 offset:%3" PGS ::"v"(p), "n"(AR), "n"(AR + 1), "n"(OFF) : "memory");
}
__device__ __forceinline__ u32 wave_shr(u32 old, u32 x) { return __builtin_amdgcn_update_dpp(old, x, 0x138, 0xf, 0xf, false); }
template <int OFF> __device__ __forceinline__ void wr2(LDS_AS char* p, u32 x, u32 y) {
  asm volatile("ds_write2_b32 %0, %1, %2 offset0:%3 offset1:%4" ::"v"(p), "v"(x), "v"(y), "n"(OFF), "n"(OFF + 1) : "memory");
}
template <int N> __device__ __forceinline__ void wait_k(u32x2 (&a)[4], u32x2 (&b)[3]) {
  asm volatile("s_waitcnt vmcnt(%7)" : "+v"(a[0]), "+v"(a[1]), "+v"(a[2]), "+v"(a[3]), "+v"(b[0]), "+v"(b[1]), "+v"(b[2]) : "n"(N) : "memory");
}
template <int N> __device__ __forceinline__ void wait_v(u32x2 (&a)[4]) {
  asm volatile("s_waitcnt vmcnt(%4)\n\ts_nop 1" : "+v"(a[0]), "+v"(a[1]), "+v"(a[2]), "+v"(a[3]) : "n"(N) : "memory");
}
template <int N> __device__ __forceinline__ void wait_bar4(u32x4 (&a)[4]) {
  asm volatile("s_waitcnt vmcnt(%4) lgkmcnt(0)\n\ts_barrier" : "+v"(a[0]), "+v"(a[1]), "+v"(a[2]), "+v"(a[3]) : "n"(N) : "memory");
}

extern "C" __global__ __launch_bounds__(256, 1) __attribute__((aligned(4096))) void h3_fused(const u16* __restrict__ q, const u16* __restrict__ k, const u16* __restrict__ v,
                                                   const u16* __restrict__ shk, const u16* __restrict__ ssk, const u16* __restrict__ ssD,
                                                   const u16* __restrict__ wg, const u16* __restrict__ wu, const u16* __restrict__ wd,
                                                   char* __restrict__ ws, u16* __restrict__ out) {
  extern __shared__ __attribute__((aligned(16))) char smraw[];
  asm volatile(PG_DEF);
  LDS_AS char* sm = (LDS_AS char*)smraw;
  const u32 smb = (u32)(size_t)sm;
  const int bid = blockIdx.x, tid = threadIdx.x, lane = tid & 63;
  const int w = __builtin_amdgcn_readfirstlane(tid >> 6);
  PROF_DECL;
  auto tcode = [&](u32 vo, u32 so) AI {
    unsigned long long pc; u32 l0, l1, l2;
    asm volatile("s_getpc_b64 %0\n.Lh3_pc0:\n\ts_mov_b32 %3, .Lh3_pc0-h3_fused\n\t"
                 "s_mov_b32 %1, .Lh3_end-h3_fused\n\ts_mov_b32 %2, .Lh3_e2-h3_fused+640\n\ts_max_i32 %1, %1, %2" : "=s"(pc), "=&s"(l0), "=&s"(l1), "=s"(l2)::"scc");
    pc -= l2;
    touch(mkrsrc((const void*)pc, l0), vo, so);
  };
  tcode(bid >= 224 && tid < 96 ? tid * 128 : OOB, (bid >> 3 & 3) * 12288);
  SCHED0();
  const int xcd = bid & 7, jb = bid >> 3;
  const u32x4 rw = mkrsrc(ws, WS_END);
  const u32 l16 = lane * 16;
  const int n = lane & 15, g = lane >> 4, qq = n >> 2, pp = lane & 3;
  const bool conv = bid < 160;
  const int cgp = 20 * xcd + jb;
  const int pm = 8 * (xcd & 1) + (jb >> 2), fn = 4 * (xcd >> 1) + (jb & 3);
  const int Hh = fn >> 3, dtile = fn & 7;
  const u32 lead = lane == 0 ? 0u : OOB;
  const u32x4 rk = mkrsrc(k, S_ * D_ * 2), rv = mkrsrc(v, S_ * D_ * 2);
  u32 tag, tagv;
  const u32 tv = (lane >> 1) * 1280 + (lane & 1) * 128 + ((160 * xcd) & ~127);
  {
    const u32 ts = ((jb - 28) * 2 + (w >> 1)) * (32 * 1280);
    asm volatile("" ::"s"(q), "s"(k), "s"(v), "s"(shk), "s"(ssk), "s"(ssD), "s"(wg), "s"(wu), "s"(wd), "s"(ws), "s"(out), "v"(tv), "s"(ts) : "a255");
    if (__builtin_expect(jb >= 28, 1)) {
#pragma unroll
      for (int i = 0; i < R_KV; ++i) touch((w & 1) ? rv : rk, tv, ts + i * (256 * 1280));
    }
  }
  const u32x4 rW = mkrsrc((w >> 1) ? wu : wg, D_ * F_ * 2);
  const u32 wso = (w & 1) * (16 * 3072) + fn * 192;
  const int hi = w >> 1, lo = w & 1;
  u32 wvo[3], ta[2][2];
  auto mkwvo = [&]() AI {
#pragma unroll
    for (int i = 0; i < 3; ++i) {
      const u32 id = i * 64 + lane, row = (id * 5462u) >> 16, pc = id - 12 * row - (w & 2);
      wvo[i] = row * 3072 + (pc & 0x80000000u ? pc + 12 : pc) * 16 - 1024 * i;
      PIN(wvo[i]);
    }
    const int p0 = 3 * w + (pp & 1) + (pp & 2), p1 = 3 * w + 2 + (pp & 2);
    const u32 rb = smb + L_S + 2 * L_SP + (pp >> 1) * 6144 + (4 * g + qq) * 192;
    const u32 t0 = rb + 16 * (p0 < 12 ? p0 : p0 - 12), t1 = rb + 16 * (p1 < 12 ? p1 : p1 - 12) + 8 * (pp & 1);
#pragma unroll
    for (int e = 0; e < 2; ++e) {
      ta[e][0] = t0 + (e ^ hi) * 12288;
      ta[e][1] = t1 + (e ^ hi) * 12288;
      PIN(ta[e][0]); PIN(ta[e][1]);
    }
  };
  auto wload = [&](auto I, auto SL) AI {
    constexpr int i = I, P = i / 6, e = (i / 3) & 1, pc = i % 3;
    dmaq<1024 * pc, pc == 0>(smb + L_S + SL * L_SP + e * 12288 + w * 3072, rW, wvo[pc], wso + (P + 10 * e) * (32 * 3072));
  };
  auto rdW = [&](auto I, auto SL) AI {
    constexpr int P = I / 12, e = (I % 12) / 6, f = (I % 6) / 2, h = I & 1;
    tr_a1<12 * (P + 10 * e) + 4 * f + 2 * h, SL * L_SP + 8 * (f == 1) + 3072 * h>(ta[e][f == 2]);
  };
  u32x4 ones = {0x3f803f80u, 0x3f803f80u, 0x3f803f80u, 0x3f803f80u};
  f32x4 cs[3];
  auto sumW = [&](auto P) AI {
    sfor<6>([&](auto I) AI { mfma_av<(P == 0 && I < 3), 12 * (P + 10 * (I / 3)) + 4 * (I % 3)>(cs[I % 3], ones); });
  };
  const u32 f3v = CT_F3 + (40 * w + (lane < 40 ? lane : 0)) * FS3;
  const u32 f3d = lane < 40 ? f3v : CT_FD + (dtile * 48 + 12 * w + (lane < 52 ? lane - 40 : 0)) * FS2;
  const u32 hsrc = WS_HT + pm * 81920 + w * 10240;
  auto ldH = [&]() AI {
    dma<4, true>(smb + w * 10240, rw, l16, hsrc);
    dma<4, true>(smb + w * 10240 + 4096, rw, l16, hsrc + 4096);
    dma<2, true>(smb + w * 10240 + 8192, rw, l16, hsrc + 8192);
  };
  if (__builtin_expect(conv, 1)) {
    const int c = cgp * 4 + w;
    u32x4 td[4];
    {
      const u32x4 rs = mkrsrc(ssk + c * S_, S_ * 2);
      sfor<4>([&](auto J) AI { ld_v<1024 * J>(td[J], rs, l16, 0); });
    }
    asm volatile("buffer_load_dword %0, off, %1, 0 sc1" : "=v"(tagv) : "s"(rw) : "memory");
    u32x8 sw;
    u32 dw;
    asm volatile("s_load_dwordx8 %0, %2, %3\n\ts_load_dword %1, %4, %5" : "=&s"(sw), "=&s"(dw) : "s"(shk), "s"(cgp * 32), "s"(ssD), "s"((c >> 1) * 4));
    u32 gv = lane * 5120 + cgp * 8, gp = lane ? OOB : (u32)((w * 256 - 3) * 1280 + cgp * 8);
    const u32 gs = w * (256 * 1280);
    u32x2 ek[4], ep[3], ev[4];
#pragma unroll
    for (int j = 0; j < 4; ++j) ld_v2(ek[j], rk, gv, gs + j * 1280);
#pragma unroll
    for (int j = 0; j < 3; ++j) ld_v2(ep[j], rk, gp, j * 1280);
#pragma unroll
    for (int j = 0; j < 4; ++j) ld_v2(ev[j], rv, gv, gs + j * 1280);
    STAMP(1);
    SCHED0();
    {
      const u32 zo = lane < 22 ? L_F + w * L_FW + l16 : lane < 44 ? L_F + w * L_FW + L_FB + l16 - 352
                   : lane < 54 ? L_G + w * L_GW + 4096 + l16 - 704 : L_G + w * L_GW + 4096 + L_GB + l16 - 864;
      *(LDS_AS u32x4*)(sm + zo) = (u32x4){0u, 0u, 0u, 0u};
      *(LDS_AS u32*)(sm + L_G + w * L_GW + 4092) = 0u;
    }
    asm volatile("s_waitcnt vmcnt(11) lgkmcnt(0)" : "+s"(sw), "+s"(dw) :: "memory");
    PIN(tagv);
    tag = __builtin_amdgcn_readfirstlane(tagv) * 0x9E3779B1u + 0x7F4A7C15u;
#pragma unroll
    for (int j = 0; j < 4; ++j) PIN(td[j]);
    {
      const u32 k0p = (pack2(bflo(td[0].x) + ((w & 1) ? bfhi(dw) : bflo(dw)), 0.f) & 0xffffu) | (td[0].x & 0xffff0000u);
      td[0].x = lane ? td[0].x : k0p;
      LDS_AS char* fo = sm + L_F + w * L_FW + 352 + l16;
      u32 last = 0;
#pragma unroll
      for (int j = 0; j < 4; ++j) {
        const u32 tp = wave_shr(last, td[j].w);
        last = __builtin_amdgcn_readlane(td[j].w, 63);
        *(LDS_AS u32x4*)(fo + L_FB + j * 1024) = td[j];
        *(LDS_AS u32x4*)(fo + j * 1024) = (u32x4){__builtin_amdgcn_alignbit(td[j].x, tp, 16), __builtin_amdgcn_alignbit(td[j].y, td[j].x, 16),
                                                  __builtin_amdgcn_alignbit(td[j].z, td[j].y, 16), __builtin_amdgcn_alignbit(td[j].w, td[j].z, 16)};
      }
    }
    const int a = n, qd = g;
    const int sg = 4 * (qd >> 1) + 2 * (a >> 3) + (qd & 1);
    LDS_AS char* fb = sm + L_F + w * L_FW + ((a & 1) ? L_FB : 0) + 32 * ((a >> 1) & 1) + 64 * ((a >> 2) & 1) + 256 * (a >> 3) + 16 * qd;
    LDS_AS char* gb = sm + L_G + w * L_GW + ((a & 1) ? L_GB : 0) + 2 * 1868 - 3584 - 8 * ((a >> 1) & 3) - 128 * (a >> 3) + 16 * qd;
    LDS_AS char* stp = sm + L_ST + l16;
    LDS_AS char* go = sm + L_G + 4088 - 512 * w - 8 * lane;
    LDS_AS char* gl = sm + L_G + w * L_GW + l16 - 4;
    LDS_AS char* gl2 = gl + 1024;
    u32 kvo = (lane >> 2) * (D_ * 64) + 16 * pp;
    u32 xo = (2 * sg + ((a >> 2) & 1)) * (D_ * 64) + 16 * (a & 3), hv = xo ^ (32 * (cgp & 1));
    u32 fsv = CT_FS + lane * FS1;
    u32 fe = w ? OOB : lane == 0 ? CT_F3 + cgp * FS3 : (lane == 1 && bid < 96) ? CT_H + bid * 128 : OOB;
    PIN(fe);
    PIN(fb); PIN(gb); PIN(stp); PIN(go); PIN(xo); PIN(hv); PIN(fsv); PIN(gl); PIN(gl2); PIN(kvo);
    mkwvo();
    STAMPA(2);
    wait_k<4>(ek, ep);
    STAMPA(3);
    float ks[4][4];
    {
      u32 kr[2][7];
#pragma unroll
      for (int i = 0; i < 7; ++i) {
        kr[0][i] = i < 3 ? wave_shr(ep[i].x, ek[i + 1].x) : ek[i - 3].x;
        kr[1][i] = i < 3 ? wave_shr(ep[i].y, ek[i + 1].y) : ek[i - 3].y;
      }
#pragma unroll
      for (int ch = 0; ch < 4; ++ch) {
        u32 P[6];
#pragma unroll
        for (int i = 0; i < 6; ++i) P[i] = __builtin_amdgcn_perm(kr[ch >> 1][i], kr[ch >> 1][i + 1], (ch & 1) ? 0x07060302u : 0x05040100u);
#pragma unroll
        for (int e = 0; e < 4; ++e) ks[ch][e] = dot4(P[e], sw[2 * ch + 1], P[e + 2], sw[2 * ch]);
      }
    }
    STAMPA(4);
    wait_v<0>(ev);
    STAMPA(5);
#pragma unroll
    for (int ch = 0; ch < 4; ++ch) {
      float o[4];
#pragma unroll
      for (int e = 0; e < 4; ++e) {
        const u32 vw = (ch >> 1) ? ev[e].y : ev[e].x;
        o[e] = ks[ch][e] * ((ch & 1) ? bfhi(vw) : bflo(vw));
      }
      const u32 x = pack2(o[3], o[2]), y = pack2(o[1], o[0]);
      *(LDS_AS u32x2*)(go + ch * L_GW + L_GB) = (u32x2){x, y};
      wr2<0>(go + ch * L_GW - 4, x, y);
    }
    asm volatile("s_cmp_lg_u32 %0, %1\n\ts_cbranch_scc1 .Lh3_p0\n\ts_mov_b64 exec, 0\n\ts_branch .Lh3_p1\n.Lh3_p0:\n\ts_mov_b64 exec, -1" ::"s"(w), "n"(PG_WA) : "scc", "memory");
    WAIT_LGKM0();
    SBAR();
    STAMP(6);
    f32x4 acc[8];
    u32x4 Ff[2][8], xv[4], kl[2];
    u32x2 Gl[2][4], Gh[2][4];
    f32x4 ql[4], xf[4], rr[2][2], mm[2][2];
    u32 hp[4], fl;
    u32x2 fa, fb2;
    auto rd = [&](auto SET, auto R, auto J, auto M0) AI {
      constexpr int set = SET, r = R, j = J, d = j < 12 ? j / 3 : j - 8, t = j < 12 ? j % 3 : 2;
      if constexpr (t == 0) Gl[set][d] = *(LDS_AS u32x2*)(gb + 3584 - 512 * (d + M0) + 64 * r);
      else if constexpr (t == 1) Gh[set][d] = *(LDS_AS u32x2*)(gb + 3584 - 512 * (d + M0) + 64 * r + 8);
      else Ff[set][d] = *(LDS_AS u32x4*)(fb + 512 * d + 64 * r);
      asm volatile("" ::: "memory");
    };
    auto rst = [&](auto MG, auto J) AI {
      constexpr int mg = MG, j = J;
      const f32x4 t = *(LDS_AS f32x4*)(stp + (mg * 4 + 2 * (j & 1) + (j >> 1)) * 1024);
      if constexpr (j & 1) mm[mg & 1][j >> 1] = t; else rr[mg & 1][j >> 1] = t;
    };
    auto prep = [&](auto MG, auto J) AI {
      constexpr int mg = MG, e = J >> 1, nb = e >> 2, reg = e & 3, b = 2 * (mg & 1) + nb;
      if constexpr ((J & 1) == 0) {
        const u32 xw = xv[mg][2 * nb + (reg >> 1)];
        float x = (reg & 1) ? bfhi(xw) : bflo(xw);
        PIN(x);
        xf[b][reg] = x;
      } else {
        float t = fmaf(xf[b][reg], rr[mg & 1][nb][reg], mm[mg & 1][nb][reg]);
        PIN(t);
        ql[b][reg] = t;
      }
      SCHED0();
    };
    auto epi = [&](auto MG, auto J) AI {
      constexpr int mg = MG, j = J, p = j / 3, b = 2 * mg + (p >> 1), e = 2 * (p & 1) + j % 3;
      if constexpr (j == 12) stc4<0>((u32x4){hp[0], hp[1], hp[2], hp[3]}, rw, hv, WS_HT + c * 64 + mg * (16 * D_ * 64));
      else if constexpr (j % 3 < 2) { float t = fmaf(ql[b][e], acc[b][e], xf[b][e]); PIN(t); xf[b][e] = t; }
      else { u32 t = pack2(xf[b][e - 2], xf[b][e - 1]); PIN(t); hp[p] = t; }
      SCHED0();
    };
    sfor<16>([&](auto J) AI { rd(IC<0>{}, IC<0>{}, J, IC<0>{}); });
    sfor<8>([&](auto R) AI {
      constexpr int r = R, cur = r & 1;
      sfor<26>([&](auto J) AI {
        constexpr int j = J, kk = PB.kk[j], m = PB.m[j], gi = 26 * r + j;
        mfma_vv<(r == 0 && first_in_phase(PB, j))>(acc[kk + m], Ff[cur][kk], mk4(Gl[cur][m], Gh[cur][m]));
        STAMPG(gi);
        if constexpr (gi == 52) STAMP(22);
        if constexpr (gi == PG_GB)
          asm volatile(PG_GO(2, 3, 4, 5) ".Lh3_p8:\n.Lh3_p9:\n.Lh3_p10:\n.Lh3_p11:\n\ts_setpc_b64 vcc\n" PG_BACK ::"s"(w) : "vcc", "scc", "memory");
        if constexpr (bar_at(gi) >= 0 && gi != G_A)
          asm volatile("s_waitcnt vmcnt(%0) lgkmcnt(0)\n\ts_barrier" ::"n"(cw(gi, C_.wl[bar_at(gi)])) : "memory");
        if constexpr (gi == G_CHK) {
          STAMP(8);
          WAITB_VM(cw(gi, C_.fl1));
          if (COLD(!allset2(fa, tag))) {
            WAITB_VM(cw(gi, C_.fl2));
            if (COLD(!allset2(fb2, tag))) spin2(rw, fsv, tag);
          }
          asm volatile("" ::"v"(fb2));
          STAMPA(9);
        }
        if constexpr (gi == G_KW) { STAMPA(7); asm volatile("s_waitcnt vmcnt(%2)" : "+v"(kl[0]), "+v"(kl[1]) : "n"(cw(gi, C_.kv)) : "memory"); STAMP(19); }
        if constexpr (gi >= G_KW && gi < G_KW + 12 && (gi - G_KW) % 2 == 0) {
          constexpr int i = (gi - G_KW) / 2, t = i / 3;
          if constexpr (i % 3 == 0) *(LDS_AS u32x4*)(gl + L_GB + 4 + 1024 * t) = kl[t];
          else wr2<2 * (i % 3 - 1)>(t ? gl2 : gl, kl[t][2 * (i % 3 - 1)], kl[t][2 * (i % 3 - 1) + 1]);
        }
        if constexpr (gi == G_A) {
          STAMP(10);
          wait_bar4<cw(gi, imax(C_.xv3, C_.wl[2]))>(xv);
          asm volatile("" ::"v"(fb2));
          STAMP(11);
        }
        if constexpr (gi == G_FL1) ldf2(fa, rw, fsv);
        if constexpr (gi == G_FL2) ldf2(fb2, rw, fsv);
        if constexpr (xs_at(gi) >= 0) {
          constexpr int i = (xs_at(gi) >> 1) - 1;
          if constexpr (i < 0) ldc_v<0>(kl[xs_at(gi)], rw, kvo, WS_KV + c * 64 + xs_at(gi) * (16 * D_ * 64));
          else if constexpr (xs_at(gi) & 1) dma<1, true>(smb + L_ST + w * 4096 + i * 1024, rw, l16, WS_ST1 + w * 4096 + i * 1024);
          else ldc_v<0>(xv[i], rw, xo, WS_XP + c * 64 + i * (16 * D_ * 64));
        }
        if constexpr (wl_at(gi) >= 0) wload(IC<wl_at(gi)>{}, IC<2 + wl_at(gi) / 6 % 3>{});
        if constexpr (wr_at(gi) >= 0) rdW(IC<wr_at(gi)>{}, IC<wr_at(gi) / 12 % 3>{});
        if constexpr (r < 7 && j >= 1 && j < 17) rd(IC<cur ^ 1>{}, IC<r + 1>{}, IC<j - 1>{}, IC<0>{});
        if constexpr (r == 7 && j >= 1 && j < 5) { rst(IC<0>{}, IC<j - 1>{}); rst(IC<1>{}, IC<j - 1>{}); }
        if constexpr (r == 7 && j >= 5 && j < 17) rd(IC<0>{}, IC<0>{}, IC<j - 5>{}, IC<4>{});
        SCHED0();
        if constexpr (r == 7 && j >= 6) {
          constexpr int b = j < 14 ? j - 6 : 2 * j - 20;
          prep(IC<b / 16>{}, IC<b % 16>{});
          if constexpr (j >= 14) prep(IC<(b + 1) / 16>{}, IC<(b + 1) % 16>{});
        }
      });
    });
    STAMP(12);
    sfor<8>([&](auto R) AI {
      constexpr int r = R, cur = r & 1;
      sfor<10>([&](auto J) AI {
        constexpr int j = J, kk = PA.kk[j], m = PA.m[j], gi = 10 * r + j;
        mfma_vv<false>(acc[4 + kk + m], Ff[cur][kk], mk4(Gl[cur][m], Gh[cur][m]));
        STAMPG(208 + gi);
        if constexpr (bar_at(208 + gi) >= 0) {
          constexpr int sq = C_.wl[bar_at(208 + gi)];
          if constexpr (208 + gi == G_B) STAMP(13);
          asm volatile("s_waitcnt vmcnt(%0) lgkmcnt(%1)\n\ts_barrier" ::"n"(cw(208 + gi, 208 + gi == G_B ? imax(sq, C_.hs) : sq)), "n"(gi ? 5 : 0) : "memory");
          if constexpr (208 + gi == G_B) { stc1(tag, rw, fe, 0); STAMP(28); }
        }
        if constexpr (wl_at(208 + gi) >= 0) wload(IC<wl_at(208 + gi)>{}, IC<2 + wl_at(208 + gi) / 6 % 3>{});
        if constexpr (wr_at(208 + gi) >= 0) rdW(IC<wr_at(208 + gi)>{}, IC<wr_at(208 + gi) / 12 % 3>{});
        if constexpr (r < 7) {
          if constexpr (j < 2) { rd(IC<cur ^ 1>{}, IC<r + 1>{}, IC<2 * j>{}, IC<4>{}); rd(IC<cur ^ 1>{}, IC<r + 1>{}, IC<2 * j + 1>{}, IC<4>{}); }
          else rd(IC<cur ^ 1>{}, IC<r + 1>{}, IC<j + 2>{}, IC<4>{});
        }
        if constexpr (r == 2 && j >= 2) rst(IC<2 + (j - 2) / 4>{}, IC<(j - 2) % 4>{});
        SCHED0();
        if constexpr (gi < 13) { epi(IC<(2 * gi) / 13>{}, IC<(2 * gi) % 13>{}); epi(IC<(2 * gi + 1) / 13>{}, IC<(2 * gi + 1) % 13>{}); }
        if constexpr (r >= 3 && r < 7 && j >= 2) prep(IC<2 + (r - 3) / 2>{}, IC<8 * ((r - 3) & 1) + j - 2>{});
      });
    });
    STAMP(14);
    asm volatile("s_waitcnt vmcnt(%4) lgkmcnt(0)\n\ts_barrier\n\ts_nop 7" : "+v"(acc[4]), "+v"(acc[5]), "+v"(acc[6]), "+v"(acc[7]) : "n"(cw(288, C_.wl[7])) : "memory");
    sfor<12>([&](auto I) AI { rdW(IC<84 + I>{}, IC<1>{}); });
#pragma unroll
    for (int m = 0; m < 2; ++m) {
      const f32x4 h0 = ql[2 * m] * acc[4 + 2 * m] + xf[2 * m];
      const f32x4 h1 = ql[2 * m + 1] * acc[5 + 2 * m] + xf[2 * m + 1];
      stc4<0>((u32x4){pack2(h0.x, h0.y), pack2(h0.z, h0.w), pack2(h1.x, h1.y), pack2(h1.z, h1.w)}, rw, hv, WS_HT + c * 64 + (m + 2) * (16 * D_ * 64));
    }
    STAMPB(2);
    asm volatile("s_nop 1" : "+v"(ones));
    sfor<6>([&](auto P) AI { wload(IC<54 + P>{}, IC<2>{}); sumW(P); });
    STAMPB(3);
    ldf(fl, rw, f3d, 0);
    sumW(IC<6>{});
    WAIT_LGKM0();
    sumW(IC<7>{});
    STAMPB(4);
    WAITB_VM(0);
    if (COLD(!allset(fl, tag))) spin(rw, f3d, 0, tag);
    STAMP(27);
    ldH();
    SBAR();
    stc1(tag, rw, lead | (w ? OOB : 0u), CT_F3 + cgp * FS3 + D3);
    STAMP(20);
    sfor<24>([&](auto I) AI { rdW(IC<96 + I>{}, IC<(I < 12 ? 2 : 0)>{}); });
    asm volatile("s_waitcnt lgkmcnt(12)" ::: "memory");
    sumW(IC<8>{});
    WAIT_LGKM0();
    sumW(IC<9>{});
    STAMP(21);
  } else {
    const int r = lane >> 3, l8 = lane & 7;
    const int ud = bid - 160;
    auto ldD = [&]() AI {
      const u32x4 rs = mkrsrc(wd, D_ * F_ * 2);
#pragma unroll
      for (int it = 0; it < 5; ++it) {
        const u32 id = (w * 5 + it) * 64 + lane, row = (id * 1639u) >> 16;
        dma<1, false>(smb + L_WD + (w * 5 + it) * 1024, rs, row * 640 + id * 16, (ud >> 1) * (32 * 1280) + (ud & 1) * 640);
      }
    };
    auto touchW = [&]() AI {
      const u32 id = ((jb >= 28 ? jb - 28 : jb - 16) * 2 + (w & 1)) * 160 + lane;
#pragma unroll
      for (int i = 0; i < 3; ++i) {
        const u32 t = id + 64 * i, p = (t * 43691u) >> 18;
        const u32 row = (p & 31) + 32 * (p >> 6) + 320 * ((p >> 5) & 1);
        touch(rW, (i < 2 || lane < 32) ? row * 3072 + (t - 6 * p) * 128 : OOB, (xcd >> 1) * 768);
      }
    };
    if (jb >= 28) {
      const int u = 255 - bid;
      u32x2 wj[10];
      {
        const u32x4 rs = mkrsrc(shk, D_ * 8);
#pragma unroll
        for (int j = 0; j < 10; ++j) ld_v2(wj[j], rs, n * 8, w * 128 + j * 512);
      }
      const u32 vt = (3 - g) * 1280 + 16 * (n ^ (2 * g)), so = (2044 - 32 * u - 8 * w) * 1280;
      auto ldkv = [&](int i) AI {
        dma<1, false>(smb + L_KK + (9 * i + 2 * w) * 1088, rk, vt, so + i * 256);
        dma<1, false>(smb + L_KK + (9 * i + 2 * w + 1) * 1088, rk, vt, so + i * 256 - 5120);
        asm volatile("s_mov_b32 m0, %0\n\ts_mov_b64 exec, %4\n\tbuffer_load_dwordx4 %1, %2, %3 offen lds\n\ts_mov_b64 exec, -1"
                     ::"s"(smb + L_KK + (9 * i + 8) * 1088), "v"(vt), "s"(rk), "s"((2012 - 32 * u) * 1280 + i * 256), "s"(0xffffull << (16 * w)) : "memory");
        dma<1, false>(smb + L_KV + (8 * i + 2 * w) * 1088, rv, vt, so + i * 256);
        dma<1, false>(smb + L_KV + (8 * i + 2 * w + 1) * 1088, rv, vt, so + i * 256 - 5120);
      };
#pragma unroll
      for (int i = 0; i < R_N0; ++i) ldkv(i);
      u32x4 Bw[10][2], Bs[2];
      LDS_AS char* kb[2];
      {
        WAIT_VM(5 * R_N0);
        const u32 sel = g < 2 ? 0x01000100u : 0x03020302u, mk = (n >> 3) == (g & 1) ? ((n & 1) ? 0xffff0000u : 0xffffu) : 0u;
        u32 mks[4];
#pragma unroll
        for (int d = 0; d < 4; ++d) mks[d] = ((n >> 1) & 3) == d ? mk : 0u;
#pragma unroll
        for (int j = 0; j < 10; ++j) {
          PIN(wj[j]);
#pragma unroll
          for (int m = 0; m < 2; ++m) {
            const u32 t = __builtin_amdgcn_perm(wj[j][m], wj[j][m], sel);
            Bw[j][m] = (u32x4){t & mks[0], t & mks[1], t & mks[2], t & mks[3]};
            PIN(Bw[j][m]);
          }
          if (R_N0 + j / 2 < 5 && (j & 1)) ldkv(R_N0 + j / 2);
        }
#pragma unroll
        for (int m = 0; m < 2; ++m) {
          const u32 t = (g >> 1) == m ? 0x3f803f80u : 0u;
          Bs[m] = (u32x4){t & mks[0], t & mks[1], t & mks[2], t & mks[3]};
          PIN(Bs[m]);
          const int rho = 8 * qq + pp + 2 * m + (g >> 1);
          kb[m] = sm + L_KK + (rho >> 2) * 1088 + (rho & 3) * 256 + 32 * (w ^ (rho & 3)) + 16 * (g & 1);
          PIN(kb[m]);
        }
      }
      asm volatile("buffer_load_dword %0, off, %1, 0 sc1" : "=v"(tagv) : "s"(rw) : "memory");
      STAMP(1);
      LDS_AS char* vb = sm + L_KV + qq * 2176 + pp * 256 + 128 * (g >> 1) + 32 * (w ^ pp) + 16 * (g & 1);
      u32 ko = n * 64 + 16 * g, kd = WS_KV + u * (D_ * 64) + w * 1024;
      PIN(vb); PIN(ko); PINS(kd);
      u32x4 A[2][4], V[2][2], o;
      f32x4 y[2][4];
      sfor<12>([&](auto J) AI {
        constexpr int j = J, i = j >> 1, p = (j & 1) ^ 1;
        if constexpr (j < 10 && (j & 1) == 0) {
          if constexpr (j == 2) STAMPA(3);
          if constexpr (j == 4) STAMPA(9);
          if constexpr (j == 8) STAMPA(14);
          WAIT_VM(5 * (4 - i) + 1 + (CT_ON == 4) + (j ? j - 2 : 0));
          SBAR();
          if constexpr (j == 0) STAMPA(2);
        }
        if constexpr (j == 11) asm volatile("s_nop 7" : "+v"(y[1][2]), "+v"(y[1][3]));
        sfor<6>([&](auto X) AI {
          constexpr int x = X;
          if constexpr (j > 0 && j < 11) {
            if constexpr (x >= 4) mfma_vv<true>(y[p][x - 2], V[(j - 1) / 2 & 1][x - 4], Bs[p]);
            else mfma_vv<(x < 2)>(y[p][x & 1], A[p][x], Bw[j - 1][x / 2]);
          }
          if constexpr (j < 10 && x < 4) A[j & 1][x] = *(LDS_AS u32x4*)(kb[x >> 1] + i * 9792 + 128 * (j & 1) + (x & 1) * 1088);
          if constexpr (j < 10 && x >= 4 && (j & 1) == 0) V[i & 1][x - 4] = *(LDS_AS u32x4*)(vb + i * 8704 + (x - 4) * 1088);
          SCHED0();
          if constexpr (j > 1) {
#pragma unroll
            for (int z = 0; z < 2; ++z) {
              if constexpr (x < 4) {
                float t = y[j & 1][x >> 1][2 * (x & 1) + z] * y[j & 1][2 + (x >> 1)][2 * (x & 1) + z];
                PIN(t);
                y[j & 1][x >> 1][2 * (x & 1) + z] = t;
              } else {
                u32 t = pack2(y[j & 1][x - 4][2 * z], y[j & 1][x - 4][2 * z + 1]);
                PIN(t);
                o[2 * (x - 4) + z] = t;
              }
            }
          }
          SCHED0();
        });
        if constexpr (j > 1) stc4<0>(o, rw, ko, kd + (j - 2) * 4096);
      });
      STAMPA(4);
      WAIT_VM(0);
      PIN(tagv);
      tag = __builtin_amdgcn_readfirstlane(tagv) * 0x9E3779B1u + 0x7F4A7C15u;
      SBAR();
      stc1(tag, rw, w || lane > 1 ? OOB : lane * FS1, CT_FS + 2 * u * FS1 + 4);
      STAMPA(5);
    } else {
      const u32x4 rq = mkrsrc(q, S_ * D_ * 2);
      const u32 vt = (16 * (g >> 1) + (g & 1)) * 1280 + 16 * (n ^ (2 * g));
      const int tl = ud;
      {
        const u32 so = (512 * (tl >> 4) + 32 * ((tl >> 1) & 7) + 8 * (tl & 1) + 2 * w) * 1280;
#pragma unroll
        for (int i = 0; i < 10; ++i)
          dma<1, false>(smb + L_PQ + (8 * (i >> 1) + 2 * w + (i & 1)) * 1088, rq, vt, so + (i & 1) * (256 * 1280) + (i >> 1) * 256);
      }
      asm volatile("buffer_load_dword %0, off, %1, 0 sc1" : "=v"(tagv) : "s"(rw) : "memory");
#pragma unroll
      for (int i = 0; i < 2; ++i) touch((w & 1) ? rv : rk, S_KV ? tv : OOB, (S_KV == 2 ? 0 : 1024 * 1280) + (512 * i + ((jb - 20) * 2 + (w >> 1)) * 32) * 1280);
      STAMP(1);
      WAIT_VM(3 + (CT_ON == 3));
      SBAR();
      STAMPA(2);
      LDS_AS char* xb = sm + L_PQ + g * 2176 + qq * 256 + 32 * (w ^ qq) + 8 * pp;
      u32x4 xa[10];
#pragma unroll
      for (int j = 0; j < 10; ++j) xa[j] = mk4(tr_read(xb + (j >> 1) * 8704 + 128 * (j & 1)), tr_read(xb + (j >> 1) * 8704 + 128 * (j & 1) + 1088));
      LDS_AS char* sp = sm + L_PQ + (2 * w + (r >> 2)) * 1088 + (r & 3) * 256 + l8 * 16;
      u32x4 d[10];
#pragma unroll
      for (int j = 0; j < 10; ++j) d[j] = *(LDS_AS u32x4*)(sp + (j >> 1) * 8704 + 128 * ((j ^ r) & 1));
      float s4[4] = {0.f, 0.f, 0.f, 0.f}, q4[4] = {0.f, 0.f, 0.f, 0.f};
#pragma unroll
      for (int j = 0; j < 10 + S_LAG; ++j) {
        if (j < 10) stc4<0>(xa[j], rw, (u32)(n * 64 + 16 * g), WS_XP + tl * (D_ * 64) + (64 * j + 16 * w) * 64);
        SCHED0();
        if (j >= S_LAG) {
#pragma unroll
          for (int e = 0; e < 4; ++e) { s4[e] = dot2(d[j - S_LAG][e], 0x3f803f80u, s4[e]); q4[e] = dot2(d[j - S_LAG][e], d[j - S_LAG][e], q4[e]); }
        }
        SCHED0();
      }
      STAMPA(3);
      float sum = (s4[0] + s4[1]) + (s4[2] + s4[3]), sq = (q4[0] + q4[1]) + (q4[2] + q4[3]);
      sum = dpp_add<0xB1>(sum); sq = dpp_add<0xB1>(sq);
      sum = dpp_add<0x4E>(sum); sq = dpp_add<0x4E>(sq);
      sum = dpp_add<0x141>(sum); sq = dpp_add<0x141>(sq);
      const float mean = sum * (1.f / D_);
      const float var = fmaxf(fmaf(-mean, mean, sq * (1.f / D_)), 0.f);
      const float den = rbf(__builtin_amdgcn_sqrtf(rbf(rbf(var) + 1e-5f)));
      const float rr = __builtin_amdgcn_rcpf(den);
      const float val = l8 ? -rbf(mean) * rr : rr;
      const int un = 4 * tl + w, sg = (un >> 3) & 7, ln = 8 * ((sg >> 1) & 1) + (un & 7) + 16 * (2 * (sg >> 2) + (sg & 1));
      stc1(__float_as_uint(val), rw, l8 < 2 ? (u32)(l8 * 2048 + (r >> 2) * 1024 + (r & 3) * 4) : OOB, WS_ST1 + (un >> 6) * 4096 + ln * 16);
      STAMPA(4);
      WAIT_VM(0);
      PIN(tagv);
      tag = __builtin_amdgcn_readfirstlane(tagv) * 0x9E3779B1u + 0x7F4A7C15u;
      SBAR();
      stc1(tag, rw, lead | (w ? OOB : 0u), CT_FS + tl * FS1);
      STAMPA(5);
    }
    touchW();
    ldD();
    mkwvo();
    WAIT_VM(0);
    PIN(tagv);
    tag = __builtin_amdgcn_readfirstlane(tagv) * 0x9E3779B1u + 0x7F4A7C15u;
    SBAR();
    STAMP(6);
    {
      u32x4 fd[5];
      LDS_AS char* p = sm + L_WD + (8 * g + qq) * 640 + 160 * w;
#pragma unroll
      for (int ii = 0; ii < 5; ++ii) {
        LDS_AS char* pi = p + 2 * (ii < 4 ? 32 * (ii >> 1) + 8 * pp + 4 * (ii & 1) : 64 + 4 * pp);
        fd[ii] = mk4(tr_read(pi), tr_read(pi + 2560));
      }
      sfor<5>([&](auto I) AI { stc4<1024 * (I & 3)>(fd[I], rw, l16, WS_WP2 + ((4 * (ud & 1) + w) * 48 + (ud >> 1)) * 5120 + (I >> 2) * 4096); });
    }
    u32 tb[2][2];
#pragma unroll
    for (int i = 0; i < 4; ++i) tb[i >> 1][i & 1] = ta[i >> 1][i & 1] - 2 * L_SP;
    asm volatile(PG_GO(e, 3, 4, 6) PG_BACK ::"s"(w) : "vcc", "scc", "memory");
    if (jb >= 28) __builtin_amdgcn_s_sleep(NC_SLP2); else __builtin_amdgcn_s_sleep(NC_SLP);
    STAMP(12);
    sfor<24>([&](auto I) AI { wload(I, IC<I / 6>{}); });
    asm volatile("" : "+v"(ones));
    u32 fh;
    sfor<10>([&](auto P) AI {
      asm volatile("s_waitcnt vmcnt(%0) lgkmcnt(0)" ::"n"(P < 7 ? 18 : P < 9 ? 6 * (9 - P) : 1) : "memory");
      if constexpr (P == 0) stc1(tag, rw, lead, CT_FD + ((4 * (ud & 1) + w) * 48 + (ud >> 1)) * FS2);
      SBAR();
      if constexpr (P == 0) STAMP(10);
      if constexpr (P == 5) STAMP(11);
      if constexpr (P == 8) ldf(fh, rw, lead, CT_H + ud * 128);
      sfor<12>([&](auto I) AI {
        constexpr int e = I / 6, f = (I % 6) / 2, h = I & 1, sl = P % 5;
        tr_a1<12 * (P + 10 * e) + 4 * f + 2 * h, (sl < 2 ? sl : sl - 2) * L_SP + 8 * (f == 1) + 3072 * h>((sl < 2 ? tb : ta)[e][f == 2]);
        if constexpr (P > 0 && (I & 1)) mfma_av<(P == 1 && I < 6), 12 * (P - 1 + 10 * (I / 6)) + 4 * ((I / 2) % 3)>(cs[(I / 2) % 3], ones);
        if constexpr (P < 6 && (I & 1)) wload(IC<6 * (P + 4) + I / 2>{}, IC<(P + 4) % 5>{});
      });
    });
    STAMP(8);
    asm volatile("s_waitcnt vmcnt(0) lgkmcnt(0)" : "+v"(fh) :: "memory");
    sumW(IC<9>{});
    STAMP(20);
    if (__builtin_amdgcn_readfirstlane(fh) != tag) {
      spin<4>(rw, lead, CT_H + ud * 128, tag);
      __builtin_amdgcn_s_sleep(H_SLP);
    }
    if (bid == 255) {
      spin(rw, (u32)lane * FS2, CT_FD + 96 * w * FS2, tag);
      spin(rw, (u32)(lane & 31) * FS2, CT_FD + (96 * w + 64) * FS2, tag);
    }
    STAMP(21);
    spin(rw, f3d, 0, tag);
    STAMP(27);
    ldH();
    stc1(tag, rw, OOB, 0);
    asm volatile(".Lh3_e2:");
  }
  asm volatile(".Lh3_pe:\n\ts_cbranch_execz .Lh3_p5");
  u32x4 Wv[NWV];
  u32x2 rs5[5];
  u32 fl, sb[2], sf;
  u32 f4v = CT_F4 + (pm * 64 + 16 * w + n) * FS4, f4s = CT_F4 + (pm * 64 + 4 * fn + w) * FS4;
  u32 bsrc = WS_HH + (8 * pm + Hh) * 49152 + 12 * w * 1024;
  u32 asrc = WS_WP2 + (dtile * 48 + 12 * w) * 5120;
  const int P = 64 * pm + 16 * Hh + 4 * w;
  u32 orow = (512 * (P >> 9) + 256 * ((P >> 2) & 1) + 32 * ((P >> 6) & 7) + 2 * ((P >> 3) & 7)) * 1280 + dtile * 160;
  u32 pbw = smb + L_PART + dtile * 5120 + w * L_PW, pbr = smb + L_PART + dtile * 5120 + 4 * w * 336;
  u32x4 ro = mkrsrc(out, S_ * D_ * 2);
  u32 vtag = tag;
  PIN(vtag); PIN(f4v); PINS(f4s); PINS(bsrc); PINS(asrc); PINS(orow); PINS(pbw); PINS(pbr); PINS(ro);
  auto ldB = [&](auto N) AI { ldc_a<112 + 4 * (N % 36), 1024 * (N & 3)>(rw, l16, sb[N / 4 & 1]); };
  auto adB = [&](auto N) AI { if constexpr (N >= 0 && (N & 3) == 0) sadd<N / 12 * 98304 + N % 12 / 4 * 4096>(sb[N / 4 & 1], bsrc); };
  {
    LDS_AS char* bp[2][2];
#pragma unroll
    for (int jj = 0; jj < 2; ++jj) {
      bp[0][jj] = sm + 256 * g + 64 * qq + 32 * (jj ^ lo ^ (g & 1)) + 8 * pp + 20480 * hi;
      bp[1][jj] = sm + 256 * g + 64 * qq + 32 * (jj ^ lo ^ (g & 1)) + 8 * pp + 20480 - 20480 * hi;
    }
    const int sto = ((dtile + 1) & 7) * 5120;
    LDS_AS char* stw0 = sm + (g == qq ? lo * 256 + n * 16 + hi * 8 : 512 + lane * 8);
    LDS_AS char* str0 = sm + ((lane >> 5) ^ lo) * 256 + n * 16;
    LDS_AS char* stw = stw0 + sto;
    LDS_AS char* str = str0 + sto;
    stw0 += 81920; str0 += 81920;
    PIN(stw); PIN(str); PIN(stw0); PIN(str0);
    u32 hv4 = ((g >> 1) ^ lo) * 49152 + (g & 1) * 256 + n * 16, hv2 = ((g >> 1) ^ lo) * 49152 + (g & 1) * 8 + n * 16;
    u32 hhs = WS_HH + 8 * pm * 49152 + (12 * fn + 3 * w) * 256;
    u32 f3l = (w | lane) || bid != 255 ? OOB : CT_E;
    PIN(hv4); PIN(hv2); PIN(f3l);
    u32 hl = smb + w * 10240, rc = w * 40960 + dtile * 5120 - 81920 - 20480 * hi, rx = 32 * (Hh ^ lo), ry = 32 * Hh;
    unsigned long long ka = 0xaaaaaaaaaaaaaaaaull, kc = 0xccccccccccccccccull;
    PINS(hhs); PINS(hl); PINS(rc); PINS(rx); PINS(ry); PINS(ka); PINS(kc);
#pragma unroll
    for (int i = 0; i < 4; ++i) PIN(bp[i >> 1][i & 1]);
    u32x2 Bl[3][2], Bh[3][2];
    f32x4 acc[2][6], sg, so, sv;
    float cst[2][5];
    u32 pk[6], sh, sa, ss, ra[3];
    float Tt[4], m0, m1, m2;
    auto swp = [&](float& x, float& y) AI {
      const auto r = __builtin_amdgcn_permlane32_swap(__float_as_uint(x), __float_as_uint(y), false, false);
      x = __uint_as_float(r[0]); y = __uint_as_float(r[1]);
    };
    u32 mi;
    auto rd = [&](auto TT, auto J) AI {
      constexpr int T = TT, j = J, q = T / 20, s = T % 20, jj = 1 - (j >> 1), t = j & 1;
      const u32x2 v = tr_read(bp[s / 10][jj] + (q & 1) * 40960 + (s % 10) * 2048 + t * 1024);
      if constexpr (t == 0) Bl[T % 3][jj] = v; else Bh[T % 3][jj] = v;
    };
    auto adH = [&](auto N) AI { if constexpr (N >= 0 && (N % 10 & 3) == 0) sadd<(N / 10 & 1) * 40960 + N / 20 * 1310720 + N % 10 / 4 * 4096>(sh, hsrc); };
    auto adA = [&](auto N) AI { if constexpr (N >= 0 && (N & 3) == 0) sadd<N / 4 * 4096>(sa, asrc); };
    auto ldA = [&](auto N) AI { ldc_a<4 * N, 1024 * (N & 3)>(rw, l16, sa); };
    constexpr float k0c = -2.f * 0.7978845608028654f * 1.4426950408889634f;
    auto op = [&](auto Q, auto ID) AI {
      constexpr int q = Q, id = ID;
      if constexpr (id < OP_SW) {
        constexpr int i = id / 8, e = id % 8 / 2, jj = id % 2, set = (q - 1) & 1;
        float x = fmaf(cs[i][e], cst[set][4 * jj], acc[set][2 * i + jj][e]);
        PIN(x);
        acc[set][2 * i + jj][e] = x;
      } else if constexpr (id < OP_CH) {
        constexpr int i = (id - OP_SW) / 4, e = (id - OP_SW) % 4, set = (q - 1) & 1;
        float x = acc[set][2 * i][e], y = acc[set][2 * i + 1][e];
        swp(x, y);
        PIN(x); PIN(y);
        acc[set][2 * i][e] = x; acc[set][2 * i + 1][e] = y;
      } else if constexpr (id < OP_PK) {
        constexpr int v = (id - OP_CH) / 8, c = (id - OP_CH) % 8, i = v / 4, e = v % 4, set = (q - 1) & 1;
        const float G = acc[set][2 * i][e];
        if constexpr (c < 6) {
          float t;
          if constexpr (c == 0) t = G * G;
          if constexpr (c == 1) t = fmaf(Tt[e], cst[set][2], cst[set][1]);
          if constexpr (c == 2) t = Tt[e] * G;
          if constexpr (c == 3) t = __builtin_amdgcn_exp2f(Tt[e]);
          if constexpr (c == 4) t = fmaf(Tt[e], cst[set][3], cst[set][3]);
          if constexpr (c == 5) t = __builtin_amdgcn_rcpf(Tt[e]);
          PIN(t);
          Tt[e] = t;
        } else {
          float u = acc[set][2 * i + 1][e] * (c == 6 ? G : Tt[e]);
          PIN(u);
          acc[set][2 * i + 1][e] = u;
        }
      } else if constexpr (id < OP_ST) {
        constexpr int t = id - OP_PK, set = (q - 1) & 1;
        u32 p = pack2(acc[set][2 * (t / 2) + 1][2 * (t & 1)], acc[set][2 * (t / 2) + 1][2 * (t & 1) + 1]);
        PIN(p);
        pk[t] = p;
      } else if constexpr (id == OP_ST) stc4n<0>((u32x4){pk[0], pk[1], pk[2], pk[3]}, rw, hv4, ss);
      else if constexpr (id == OP_ST + 1) stc2<512>((u32x2){pk[4], pk[5]}, rw, hv2, ss);
      else if constexpr (id < OP_MATH) {
        constexpr int e = id - OP_EXT;
        if constexpr (e == 0) asm volatile("v_cndmask_b32_e64 %0, %1, %2, %3" : "=v"(m0) : "v"(sg.x), "v"(sg.y), "s"(ka), "v"(so));
        if constexpr (e == 1) asm volatile("v_cndmask_b32_e64 %0, %1, %2, %3" : "=v"(m1) : "v"(sg.z), "v"(sg.w), "s"(ka));
        if constexpr (e == 2) asm volatile("v_cndmask_b32_e64 %0, %1, %2, %3" : "=v"(so.y) : "v"(m0), "v"(m1), "s"(kc));
        if constexpr (e == 3) *(LDS_AS f32x2*)(q == 0 ? stw0 : stw + (q - 1) * 1024) = (f32x2){so.x, so.y};
      } else if constexpr (id < OP_RA) {
        constexpr int m = id - OP_MATH, set = q & 1;
        if constexpr (m == 0) { m0 = sv.x + sv.z; PIN(m0); }
        if constexpr (m == 1) { m1 = sv.y + sv.w; PIN(m1); }
        if constexpr (m == 2) { m0 = m0 * (-1.f / D_); PIN(m0); }
        if constexpr (m == 3) { m1 = m1 * (1.f / D_); PIN(m1); }
        if constexpr (m == 4) { m1 = fmaf(-m0, m0, m1); PIN(m1); }
        if constexpr (m == 5) { m1 = __builtin_amdgcn_fmed3f(m1, 0.f, 3.0e38f); PIN(m1); }
        if constexpr (m == 6) { mi = pack2(m0, m1); PIN(mi); }
        if constexpr (m == 7) { m0 = bflo(mi); PIN(m0); cst[set][0] = m0; }
        if constexpr (m == 8) { m2 = bflo(mi); PIN(m2); cst[set][4] = m2; }
        if constexpr (m == 9) { m1 = bfhi(mi); PIN(m1); }
        if constexpr (m == 10) { m1 = m1 + 1e-5f; PIN(m1); }
        if constexpr (m == 11) { mi = pack2(m1, m1); PIN(mi); }
        if constexpr (m == 12) { m1 = bflo(mi); PIN(m1); }
        if constexpr (m == 13) { m1 = __builtin_amdgcn_sqrtf(m1); PIN(m1); }
        if constexpr (m == 14) { swp(cst[set][0], cst[set][4]); PIN(cst[set][0]); PIN(cst[set][4]); }
        if constexpr (m == 15) { mi = pack2(m1, m1); PIN(mi); }
        if constexpr (m == 16) { m1 = bflo(mi); PIN(m1); }
        if constexpr (m == 17) { m2 = __builtin_amdgcn_rcpf(m1); PIN(m2); }
        if constexpr (m == 18) { m0 = m1 * m1; PIN(m0); cst[set][3] = m0; }
        if constexpr (m == 19) { m1 = m2 * k0c; PIN(m1); cst[set][1] = m1; }
        if constexpr (m == 20) { m2 = m2 * m2; PIN(m2); }
        if constexpr (m == 21) { m2 = m2 * 0.044715f; PIN(m2); }
        if constexpr (m == 22) { m2 = m2 * m1; PIN(m2); cst[set][2] = m2; }
      } else {
        constexpr int m = id - OP_RA;
        if constexpr (m == 0) { ra[2] = (u32)(size_t)bp[0][0] ^ rx; PIN(ra[2]); }
        if constexpr (m == 1) { ra[2] += rc; PIN(ra[2]); }
        if constexpr (m == 2) { ra[0] = l16 & 0x300; PIN(ra[0]); }
        if constexpr (m == 3) { ra[0] += ra[2]; PIN(ra[0]); }
        if constexpr (m == 4) { asm volatile("v_bfi_b32 %0, 32, %1, %0" : "+v"(ra[0]) : "s"(ry)); }
        if constexpr (m == 5) { ra[1] = ra[0] ^ 32; PIN(ra[1]); }
      }
    };
    adH(IC<10>{});
    WAIT_VM(wq(0, Q_.hd[0]));
    SBAR();
    STAMP(15);
    sfor<4>([&](auto J) AI { rd(IC<0>{}, J); asm volatile("" ::: "memory"); });
    sfor<4>([&](auto J) AI { rd(IC<1>{}, J); asm volatile("" ::: "memory"); });
    sfor<4>([&](auto Q) AI {
      sfor<20>([&](auto S) AI {
        constexpr int q = Q, s = S, T = 20 * q + s, nm = nm_of(T), cur = T % 3;
        sfor<nm>([&](auto X) AI {
          constexpr int x = X;
          const u32x4 B = mk4(Bl[cur][x & 1 & (x < 6)], Bh[cur][x & 1 & (x < 6)]);
          if constexpr (x < 6) mfma_av<s == 0, 12 * s + 4 * (x >> 1)>(acc[q & 1][x], B);
          else if constexpr (x == 6) mfma_vv<s == 0>(sg, B, B);
          else mfma_vv<s == 0>(so, ones, B);
          constexpr int slot = 8 * T + x;
          if constexpr (x == 0 && T >= 70) STAMPT(T - 69);
          if constexpr (x == nm - 1 && T < 79) WAITB_LG(T == 78 ? 2 : nm == 8 ? 4 : 3);
          if constexpr (x == 0 && hd_at(T, 1) >= 0 && (hd_at(T, 1) % 10 & 3) == 0) sm0<hd_at(T, 1) / 10 * 40960 + hd_at(T, 1) % 10 / 4 * 4096>(hl);
          if constexpr (x == 2) adH(IC<hd_at(T + 1, 1)>{});
          adA(IC<wv_at(T, x + 1)>{});
          if constexpr (wa_at(T, x + 1) >= 0) adA(IC<NWV + wa_at(T, x + 1)>{});
          adB(IC<hb_at(T, x + 1)>{});
          if constexpr (x == 3 && s == 1 && q > 0) sadd<(q - 1) * 98304>(ss, hhs);
          if constexpr (x == 3 && (fs_at(T, 6) == 0 || fs_at(T, 6) == 1)) sadd<D4 * fs_at(T, 6)>(sf, f4s);
          if constexpr (fl_at(T, x + 1) == 4) smov<D3>(sf);
          if constexpr (fl_at(T, x + 1) == 1) smov<D4>(sf);
          if constexpr (x == 1) {
            if constexpr (T == 18 || T == 38) { WAIT_VM(wq(slot, Q_.hd[q + 1])); SBAR(); }
            if constexpr (s == 13) {
              if constexpr (q == 2) WAIT_VM(wq(slot, Q_.hd[3]));
              SBAR();
              sv = *(LDS_AS f32x4*)(q == 0 ? str0 : str + (q - 1) * 1024);
            }
            if constexpr (T == T_FB) {
              WAITB_VM(wq(slot, Q_.fl[4]));
              if (COLD(!allset(fl, tag))) spin(rw, f3v, D3, tag);
            }
            if constexpr (T == 71) {
              WAITB_VM(wq(slot, Q_.fl[0]));
              if (COLD(!allset(fl, tag))) spin(rw, f4v, 0, tag);
            }
          }
          if constexpr (fs_at(T, x) == 4) stc1(vtag, rw, f3l, 0);
          else if constexpr (fs_at(T, x) >= 0) { WAIT_VM(wq(slot, Q_.st[fs_at(T, x)])); stc1(vtag, rw, lead, sf); }
          if constexpr (fl_at(T, x) == 4) ldf(fl, rw, f3v, sf);
          if constexpr (fl_at(T, x) == 0) ldf(fl, rw, f4v, 0);
          if constexpr (fl_at(T, x) == 1) ldf(fl, rw, f4v, sf);
          if constexpr (hd_at(T, x) >= 0) dmap<1024 * (hd_at(T, x) % 10 & 3), false>(0, rw, l16, sh);
          if constexpr (wv_at(T, x) >= 0) ldc_v<1024 * (wv_at(T, x) & 3)>(Wv[wv_at(T, x)], rw, l16, sa);
          if constexpr (wa_at(T, x) >= 0) ldA(IC<wa_at(T, x)>{});
          if constexpr (hb_at(T, x) >= 0) ldB(IC<hb_at(T, x)>{});
          if constexpr (ds_at(T, x) && T < 78) rd(IC<T + 2>{}, IC<x - 2>{});
          if constexpr (ds_at(T, x) && T >= 78) {
            constexpr int i = T == 78 ? x / 2 - 1 : x < 4 ? x + 1 : 4;
            rs5[i] = tr_read((LDS_AS char*)(size_t)ra[i < 4 ? i & 1 : 2] + (i < 4 ? 2048 * (i >> 1) + 256 * (i & 1) : 4096));
          }
          SCHED0();
          if constexpr (x == 0 && (T == 30 || T == 40)) {
            constexpr int hs = T == 40;
            bp[hs][0] += 81920; bp[hs][1] += 81920;
            asm volatile("" : "+v"(bp[hs][0]), "+v"(bp[hs][1]));
          }
          sfor<KS[q].n[8 * s + x]>([&](auto O) AI { op(Q, IC<KS[q].op[8 * s + x][O]>{}); SCHED0(); });
          SCHED0();
        });
      });
      if constexpr (Q != 3) STAMP(16 + Q);
    });
    STAMPT(11);
    ldA(IC<NWA>{});
    asm volatile("s_nop 5" : "+v"(acc[1][0]), "+v"(acc[1][1]), "+v"(acc[1][2]), "+v"(acc[1][3]), "+v"(acc[1][4]), "+v"(acc[1][5]));
    sadd<3 * 98304>(ss, hhs);
    sadd<2 * D4>(sf, f4s);
    sfor<6>([&](auto TT) AI {
      constexpr int t = TT, i = t >> 1, e = 2 * (t & 1);
      const f32x2 cc = {cs[i][e], cs[i][e + 1]};
      const f32x2 X = cc * cst[1][0] + (f32x2){acc[1][2 * i][e], acc[1][2 * i][e + 1]};
      const f32x2 Y = cc * cst[1][4] + (f32x2){acc[1][2 * i + 1][e], acc[1][2 * i + 1][e + 1]};
      float g0 = X.x, g1 = X.y, u0 = Y.x, u1 = Y.y;
      swp(g0, u0); swp(g1, u1);
      const float K0 = cst[1][1], K1 = cst[1][2], ir2 = cst[1][3];
      const f32x2 G = {g0, g1}, U = {u0, u1};
      const f32x2 wv = ((G * G) * K1 + K0) * G;
      f32x2 ex; ex.x = __builtin_amdgcn_exp2f(wv.x); ex.y = __builtin_amdgcn_exp2f(wv.y);
      const f32x2 dd = ex * ir2 + ir2;
      f32x2 rc; rc.x = __builtin_amdgcn_rcpf(dd.x); rc.y = __builtin_amdgcn_rcpf(dd.y);
      const f32x2 hv = (G * U) * rc;
      pk[t] = pack2(hv.x, hv.y);
      PIN(pk[t]);
      if constexpr (t == 3) stc4<0>((u32x4){pk[0], pk[1], pk[2], pk[3]}, rw, hv4, ss);
      if constexpr (t == 5) stc2<512>((u32x2){pk[4], pk[5]}, rw, hv2, ss);
      if constexpr (t == TL_FS) { WAIT_VM(wq(640 + t, Q_.st[2])); stc1(vtag, rw, lead, sf); }
      adA(IC<NWV + NWA + 1 + t>{});
      ldA(IC<NWA + 1 + t>{});
    });
    STAMP(23);
    STAMPT(12);
    adB(IC<12>{});
    WAITB_VM(wq(646, imax(Q_.fl[1], need3(0))));
    STAMPT(13);
    if (COLD(!allset(fl, tag))) spin(rw, f4v, D4, tag);
    STAMPT(14);
  }
  {
    f32x4 acc[2][5], rz[5], pt[4][2];
    u32 o4[4], pwa, pwb, pr, ov, rl, cl, so;
    auto wpart = [&](auto S, auto I) AI {
      constexpr int s = S, i = I;
      if constexpr (i < 4) *(LDS_AS f32x4*)((LDS_AS char*)(size_t)pwa + 4 * (s & 1) * L_PW + 128 * (i >> 1) + 16 * (i & 1)) = acc[s & 1][i];
      else *(LDS_AS f32x4*)((LDS_AS char*)(size_t)pwb + 4 * (s & 1) * L_PW) = acc[s & 1][i];
    };
    auto rpart = [&](auto S, auto J) AI { pt[J & 3][J >> 2] = *(LDS_AS f32x4*)((LDS_AS char*)(size_t)pr + (4 * (S & 1) + (J & 3)) * L_PW + 16 * (J >> 2)); };
    auto res = [&](auto S, auto J) AI {
      const u32 x = rs5[J / 4][J % 4 / 2];
      u32 r;
      if constexpr (J & 1) r = x & (w == S ? 0xffff0000u : 0u);
      else r = __builtin_amdgcn_perm(x, x, w == S ? 0x01000c0cu : 0x0c0c0c0cu);
      PIN(r);
      return __uint_as_float(r);
    };
    auto op3 = [&](auto ID) AI {
      constexpr int id = ID;
      if constexpr (id == 0) { pwb = __umul24(n, 336) + pbw; PIN(pwb); }
      if constexpr (id == 1) { rl = __umul24(lane, 26); PIN(rl); }
      if constexpr (id == 2) { pwa = pwb + 32 * g; PIN(pwa); }
      if constexpr (id == 3) { rl >>= 8; PIN(rl); }
      if constexpr (id == 4) { pwb += 16 * g; PIN(pwb); }
      if constexpr (id == 5) { cl = l16 - __umul24(rl, 160); PIN(cl); }
      if constexpr (id == 6) { pr = __umul24(rl, 336) + pbr; PIN(pr); }
      if constexpr (id == 7) { ov = __umul24(rl, 1280) + cl; PIN(ov); }
      if constexpr (id == 8) { pr += 2 * cl; PIN(pr); }
      if constexpr (id == 9) { rl >>= 1; PIN(rl); }
      if constexpr (id == 10) { pwb += 256; PIN(pwb); }
      if constexpr (id == 11) { ov += __umul24(rl, 14 * 1280); ov = lane < 40 ? ov : OOB; PIN(ov); }
      if constexpr (id >= O3_CV && id < O3_PA) rz[(id - O3_CV) / 4][(id - O3_CV) % 4] = res(IC<0>{}, IC<id - O3_CV>{});
      if constexpr (id >= O3_PA && id < O3_IN) {
        constexpr int t = (id - O3_PA) / 4, e = (id - O3_PA) % 4;
        float x = acc[0][t][e] + rz[t][e];
        PIN(x);
        acc[0][t][e] = x;
      }
      if constexpr (id >= O3_IN && id < O3_AD) {
        constexpr int s = (id - O3_IN) / 20 + 1, j = (id - O3_IN) % 20;
        acc[s & 1][j / 4][j % 4] = res(IC<s>{}, IC<j>{});
      }
      if constexpr (id >= O3_AD) {
        constexpr int a = (id - O3_AD) % 28, h = a / 14, b = a % 14, e = b & 3;
        if constexpr (b < 12) {
          constexpr int u = b < 8 ? 2 * (b >> 2) : 0, v = b < 8 ? u + 1 : 2;
          float x = pt[u][h][e] + pt[v][h][e];
          PIN(x);
          pt[u][h][e] = x;
        } else {
          u32 p = pack2(pt[0][h][2 * e], pt[0][h][2 * e + 1]);
          PIN(p);
          o4[2 * h + e] = p;
        }
      }
    };
    sfor<48>([&](auto TT) AI {
      constexpr int T = TT, s = T / 12, k = T % 12;
      if constexpr (T < 6) STAMPT(15 + T);
      if constexpr (T >= 12 && T < 20) STAMPT(9 + T);
      sfor<5>([&](auto I) AI {
        constexpr int i = I, na = 5 * k + i, br = 112 + 4 * (T % 36), G = 5 * T + i, slot = 648 + G;
        if constexpr (na < NWV) mfma_va<T == 0, br>(acc[s & 1][i], Wv[na]);
        else mfma_aa<false, 4 * (na - NWV), br>(acc[s & 1][i]);
        if constexpr (i == 4 && T < 47) {
          constexpr int v = wq(slot, need3(T + 1)), l = T == 0 ? 0 : s == 0 ? 15 : k == 5 || k == 8 ? 0 : k == 7 ? 4 : 15;
          __builtin_amdgcn_s_waitcnt((v & 15) | 0x70 | (l << 8) | ((v >> 4) << 14));
          if constexpr (s >= 1 && k == 5) SBAR();
        }
        if constexpr (i == 2 && T < 47) adB(IC<hb3_at(T + 1, 0)>{});
        if constexpr (i == 1 && T + 1 == P3_FS) sadd<3 * D4>(so, f4s);
        if constexpr (i == 1 && T + 1 == P3_L2) smov<2 * D4>(sf);
        if constexpr (i == 1 && T + 1 == P3_L3) smov<3 * D4>(sf);
        if constexpr (i == 1 && s >= 1 && k == 10) sadd<(1024 * ((s - 1) >> 1) + 8 * ((s - 1) & 1)) * 1280>(so, orow);
        if constexpr (T == P3_FS && i == 1) { WAIT_VM(wq(slot, Q_.st[3])); stc1(vtag, rw, lead, so); }
        if constexpr ((T == P3_L2 || T == P3_L3) && i == 1) ldf(fl, rw, f4v, sf);
        if constexpr ((T == P3_C2 || T == P3_C3) && i == 0) {
          WAITB_VM(wq(slot, Q_.fl[T == P3_C2 ? 2 : 3]));
          if (COLD(!allset(fl, tag))) spin(rw, f4v, D4 * (T == P3_C2 ? 2 : 3), tag);
        }
        if constexpr (hb3_at(T, i) >= 0) ldB(IC<hb3_at(T, i)>{});
        if constexpr (s >= 1 && k < 5 && i == 2) wpart(IC<s - 1>{}, IC<k>{});
        if constexpr (rd3_at(G) >= 0) rpart(IC<s - 1>{}, IC<rd3_at(G)>{});
        if constexpr (s >= 1 && k == 11 && i == 2) st_out((u32x4){o4[0], o4[1], o4[2], o4[3]}, ro, ov, so);
        SCHED0();
        sfor<K3.n[G]>([&](auto O) AI { op3(IC<K3.op[G][O]>{}); SCHED0(); });
      });
      if constexpr (k == 11 && s > 0) STAMP(23 + s);
    });
    asm volatile("s_nop 7" : "+v"(acc[1][0]), "+v"(acc[1][1]), "+v"(acc[1][2]), "+v"(acc[1][3]), "+v"(acc[1][4]));
    sfor<5>([&](auto I) AI { wpart(IC<3>{}, I); });
    WAIT_LGKM0();
    SBAR();
    sfor<8>([&](auto J) AI { rpart(IC<3>{}, J); });
#pragma unroll
    for (int h = 0; h < 2; ++h) {
      const f32x4 t = (pt[0][h] + pt[1][h]) + (pt[2][h] + pt[3][h]);
      o4[2 * h] = pack2(t.x, t.y); o4[2 * h + 1] = pack2(t.z, t.w);
    }
    st_out((u32x4){o4[0], o4[1], o4[2], o4[3]}, ro, ov, orow + (1024 + 8) * 1280);
  }
  PROF_END();
  asm volatile(".Lh3_end:");
}

extern "C" void kernel_launch(void* const* d_in, const int* in_sizes, int n_in, void* d_out, int out_size, void* d_ws,
                              size_t ws_size, hipStream_t stream) {
  (void)hipFuncSetAttribute((const void*)h3_fused, hipFuncAttributeMaxDynamicSharedMemorySize, LDS_BYTES);
  h3_fused<<<256, 256, LDS_BYTES, stream>>>((const u16*)d_in[0], (const u16*)d_in[1], (const u16*)d_in[2], (const u16*)d_in[3],
                                            (const u16*)d_in[4], (const u16*)d_in[5], (const u16*)d_in[7], (const u16*)d_in[8],
                                            (const u16*)d_in[9], (char*)d_ws, (u16*)d_out);
}
```
